```python
import torch

def _softmax(x, axis=-1):
    return torch.softmax(x, dim=axis)
def _gelu(x):
    return torch.nn.functional.gelu(x, approximate="tanh")
def _silu(x):
    return torch.nn.functional.silu(x)
def _sigmoid(x):
    return torch.sigmoid(x)
def _layernorm(x, axis=-1, eps=1e-5):
    m = x.mean(dim=axis, keepdim=True)
    v = x.var(dim=axis, keepdim=True, unbiased=False)
    return (x - m) / torch.sqrt(v + eps)
def _scatter_add(tgt, idx, src, axis=0):
    return tgt.clone().index_add_(axis, idx, src)
def _segment_sum(src, seg, n):
    out = torch.zeros((n,) + src.shape[1:], dtype=src.dtype)
    return out.index_add_(0, seg, src)
def _pad(x, pw):
    flat = tuple(v for pair in reversed(pw) for v in pair)
    return torch.nn.functional.pad(x, flat)

kernel_name = 'parallel_sum__ssm_conv_h3__glu_gated_reglu__prompt'

def setup_inputs():

    _w = (lambda a: (lambda t: t.to(torch.bfloat16) if t.is_floating_point() else t)(torch.as_tensor(a)))
    def _A_setup():
        B, S, D = 1, 2048, 512
        ks = 4
        return {
            "q": _w((lambda *s: torch.randn(*s, dtype=torch.bfloat16) * 0.05)(B, S, D)),
            "k": _w((lambda *s: torch.randn(*s, dtype=torch.bfloat16) * 0.2)(B, S, D)),
            "v": _w((lambda *s: torch.randn(*s, dtype=torch.bfloat16) * 0.2)(B, S, D)),
            "shift_k": _w((lambda *s: torch.randn(*s, dtype=torch.bfloat16) * 0.2)(D, ks) * 0.5),
            "ssm_k": _w((lambda *s: torch.randn(*s, dtype=torch.bfloat16) * 0.4)(D, S) * (1.0 / S ** 0.5)),
            "ssm_D": _w((lambda *s: torch.randn(*s, dtype=torch.bfloat16) * 0.2)(D)),
        }
    _A_in = _A_setup()

    def _B_setup():
        B, S, D, F = 1, 2048, 512, 1792
        return {
            "x": _w((lambda *s: torch.randn(*s, dtype=torch.bfloat16) * 0.05)(B, S, D)),
            "W_gate": _w((lambda *s: torch.randn(*s, dtype=torch.bfloat16) * 0.2)(D, F) * (2.0 / D) ** 0.5),
            "W_up": _w((lambda *s: torch.randn(*s, dtype=torch.bfloat16) * 0.2)(D, F) * (2.0 / D) ** 0.5),
            "W_down": _w((lambda *s: torch.randn(*s, dtype=torch.bfloat16) * 0.2)(F, D) * (1.0 / F) ** 0.5),
        }
    _B_in = _B_setup()

    return {
        "A_q": _A_in["q"],
        "A_k": _A_in["k"],
        "A_v": _A_in["v"],
        "A_shift_k": _A_in["shift_k"],
        "A_ssm_k": _A_in["ssm_k"],
        "A_ssm_D": _A_in["ssm_D"],
        "B_x": _B_in["x"],
        "B_W_gate": _B_in["W_gate"],
        "B_W_up": _B_in["W_up"],
        "B_W_down": _B_in["W_down"],
    }

def reference(A_q, A_k, A_v, A_shift_k, A_ssm_k, A_ssm_D, B_x, B_W_gate, B_W_up, B_W_down):
    def _A_ref(q, k, v, shift_k, ssm_k, ssm_D):
        B, S, D = q.shape
        ks = shift_k.shape[-1]
        k_pad = _pad(k, ((0, 0), (ks - 1, 0), (0, 0)))
        k_shift = torch.zeros_like(k)
        for t in range(ks):
            k_shift = k_shift + k_pad[:, t:t + S, :] * shift_k[:, ks - 1 - t][None, None, :]
        kv = k_shift * v
        L = 2 * S
        kv_pad = _pad(kv, ((0, 0), (0, S), (0, 0)))
        kf_pad = _pad(ssm_k, ((0, 0), (0, S)))
        kv_f = (lambda x, axes=(-2,-1): torch.fft.rfft2(x.float(), dim=axes))(kv_pad[:, None, :, :], axes=(1, 2))
        kf_f = (lambda x, axes=(-2,-1): torch.fft.rfft2(x.float(), dim=axes))(kf_pad[:, None, :], axes=(1, 2))
        kf_f = (lambda x, *d: x.permute(*d))(kf_f, 1, 2, 0)
        y_f = kv_f * kf_f[None, :, :, :]
        y = (lambda x, s, axes=(-2,-1): torch.fft.irfft2(x, s=s, dim=axes).to(torch.bfloat16))(y_f, (1, L), axes=(1, 2))[:, 0, :S, :]
        y = y + kv * ssm_D[None, None, :]
        return q * y

    def _B_ref(x, W_gate, W_up, W_down):
        gate = torch.einsum("bsd,df->bsf", x, W_gate)
        up = torch.einsum("bsd,df->bsf", x, W_up)
        gate = gate * (lambda x, dt: x.to(dt))(gate > 0, gate.dtype)
        h = gate * up
        return torch.einsum("bsf,fd->bsd", h, W_down)

    x = A_q
    xn = _layernorm(x, axis=-1)
    return x + _A_ref(xn, A_k, A_v, A_shift_k, A_ssm_k, A_ssm_D) + _B_ref(xn, B_W_gate, B_W_up, B_W_down)
```

```cpp
#include <hip/hip_runtime.h>
#include <hip/hip_bf16.h>
#include <stdint.h>
typedef unsigned short u16;
static __device__ __forceinline__ float bf2f(u16 v){ return __uint_as_float(((uint32_t)v)<<16); }
static __device__ __forceinline__ u16 f2bf(float f){ __hip_bfloat16 b = __float2bfloat16(f); return *(u16*)&b; }
static __device__ __forceinline__ float rbf(float f){ return bf2f(f2bf(f)); }
constexpr int S=2048, D=512, F=1792;
__global__ void k_prep(const u16* q,const u16* k,const u16* v,const u16* sk,u16* xn,u16* kvT){
  int t=blockIdx.x; int d=threadIdx.x; __shared__ float red[512];
  float x=bf2f(q[t*D+d]); red[d]=x; __syncthreads();
  for(int o=256;o>0;o>>=1){ if(d<o) red[d]+=red[d+o]; __syncthreads(); }
  float m=red[0]/D; __syncthreads(); red[d]=(x-m)*(x-m); __syncthreads();
  for(int o=256;o>0;o>>=1){ if(d<o) red[d]+=red[d+o]; __syncthreads(); }
  float var=red[0]/D;
  xn[t*D+d]=f2bf((x-m)/sqrtf(var+1e-5f));
  float ks=0; for(int j=0;j<4;j++){ int tt=t-3+j; float kk= tt>=0? bf2f(k[tt*D+d]):0.f; ks+= kk*bf2f(sk[d*4+3-j]); }
  kvT[d*S+t]=f2bf(rbf(ks)*bf2f(v[t*D+d]));
}
__global__ void k_g1(const u16* xn,const u16* wg,const u16* wu,u16* h){
  int t=blockIdx.x; __shared__ float xs[512]; for(int i=threadIdx.x;i<D;i+=256) xs[i]=bf2f(xn[t*D+i]); __syncthreads();
  for(int f=threadIdx.x;f<F;f+=256){ float g=0,u=0; for(int d=0;d<D;d++){ g+=xs[d]*bf2f(wg[d*F+f]); u+=xs[d]*bf2f(wu[d*F+f]);} h[t*F+f]=f2bf((g>0?g:0.f)*u);} }
__global__ void k_conv(const u16* kvT,const u16* ssm,float* yT){
  int d=blockIdx.x; __shared__ float us[S], hs[S]; for(int i=threadIdx.x;i<S;i+=256){us[i]=bf2f(kvT[d*S+i]); hs[i]=bf2f(ssm[d*S+i]);} __syncthreads();
  for(int t=threadIdx.x;t<S;t+=256){ float a=0; for(int s=0;s<=t;s++) a+=hs[t-s]*us[s]; yT[d*S+t]=a; } }
__global__ void k_g2(const u16* q,const u16* xn,const u16* h,const u16* wd,const u16* kvT,const float* yT,const u16* sd,u16* out){
  int t=blockIdx.x; __shared__ float hs[F]; for(int i=threadIdx.x;i<F;i+=256) hs[i]=bf2f(h[t*F+i]); __syncthreads();
  for(int d=threadIdx.x;d<D;d+=256){ float a=0; for(int f=0;f<F;f++) a+=hs[f]*bf2f(wd[f*D+d]);
    float y=yT[d*S+t]+bf2f(kvT[d*S+t])*bf2f(sd[d]); out[t*D+d]=f2bf(bf2f(q[t*D+d])+bf2f(xn[t*D+d])*y+a);} }
extern "C" void kernel_launch(void* const* d_in, const int* in_sizes, int n_in, void* d_out, int out_size, void* d_ws, size_t ws_size, hipStream_t stream) {
  const u16* q=(const u16*)d_in[0]; const u16* k=(const u16*)d_in[1]; const u16* v=(const u16*)d_in[2]; const u16* sk=(const u16*)d_in[3];
  const u16* ssm=(const u16*)d_in[4]; const u16* sd=(const u16*)d_in[5]; const u16* wg=(const u16*)d_in[7]; const u16* wu=(const u16*)d_in[8]; const u16* wd=(const u16*)d_in[9];
  char* ws=(char*)d_ws; u16* xn=(u16*)ws; u16* kvT=(u16*)(ws+(2<<20)); float* yT=(float*)(ws+(4<<20)); u16* h=(u16*)(ws+(8<<20));
  k_prep<<<S,512,0,stream>>>(q,k,v,sk,xn,kvT);
  k_g1<<<S,256,0,stream>>>(xn,wg,wu,h);
  k_conv<<<D,256,0,stream>>>(kvT,ssm,yT);
  k_g2<<<S,256,0,stream>>>(q,xn,h,wd,kvT,yT,sd,(u16*)d_out);
}
```

```cpp
#include <hip/hip_runtime.h>
#include <hip/hip_bf16.h>
#include <stdint.h>
typedef unsigned short u16;
static __device__ __forceinline__ float bf2f(u16 v){ return __uint_as_float(((uint32_t)v)<<16); }
static __device__ __forceinline__ u16 f2bf(float f){ __hip_bfloat16 b = __float2bfloat16(f); return *(u16*)&b; }
static __device__ __forceinline__ float rbf(float f){ return bf2f(f2bf(f)); }
constexpr int S=2048, D=512, F=1792;
__global__ void k_prep(const u16* q,const u16* k,const u16* v,const u16* sk,u16* xn,u16* kvT){
  int t=blockIdx.x; int d=threadIdx.x; __shared__ float red[512];
  float x=bf2f(q[t*D+d]); red[d]=x; __syncthreads();
  for(int o=256;o>0;o>>=1){ if(d<o) red[d]+=red[d+o]; __syncthreads(); }
  float m=red[0]/D; __syncthreads(); red[d]=(x-m)*(x-m); __syncthreads();
  for(int o=256;o>0;o>>=1){ if(d<o) red[d]+=red[d+o]; __syncthreads(); }
  float var=red[0]/D;
  xn[t*D+d]=f2bf((x-m)/sqrtf(var+1e-5f));
  float ks=0; for(int j=0;j<4;j++){ int tt=t-3+j; float kk= tt>=0? bf2f(k[tt*D+d]):0.f; ks+= kk*bf2f(sk[d*4+3-j]); }
  kvT[d*S+t]=f2bf(rbf(ks)*bf2f(v[t*D+d]));
}
__global__ void k_g1(const u16* xn,const u16* wg,const u16* wu,u16* h){
  int t=blockIdx.x; __shared__ float xs[512]; for(int i=threadIdx.x;i<D;i+=256) xs[i]=bf2f(xn[t*D+i]); __syncthreads();
  for(int f=threadIdx.x;f<F;f+=256){ float g=0,u=0; for(int d=0;d<D;d++){ g+=xs[d]*bf2f(wg[d*F+f]); u+=xs[d]*bf2f(wu[d*F+f]);} h[t*F+f]=f2bf((g>0?g:0.f)*u);} }
__global__ void k_conv(const u16* kvT,const u16* ssm,float* yT){
  int d=blockIdx.x; __shared__ float us[S], hs[S]; for(int i=threadIdx.x;i<S;i+=256){us[i]=bf2f(kvT[d*S+i]); hs[i]=bf2f(ssm[d*S+i]);} __syncthreads();
  for(int t=threadIdx.x;t<S;t+=256){ float a=0; for(int s=0;s<=t;s++) a+=hs[t-s]*us[s]; yT[d*S+t]=a; } }
__global__ void k_g2(const u16* q,const u16* xn,const u16* h,const u16* wd,const u16* kvT,const float* yT,const u16* sd,u16* out){
  int t=blockIdx.x; __shared__ float hs[F]; for(int i=threadIdx.x;i<F;i+=256) hs[i]=bf2f(h[t*F+i]); __syncthreads();
  for(int d=threadIdx.x;d<D;d+=256){ float a=0; for(int f=0;f<F;f++) a+=hs[f]*bf2f(wd[f*D+d]);
    float y=yT[d*S+t]+bf2f(kvT[d*S+t])*bf2f(sd[d]); out[t*D+d]=f2bf(bf2f(q[t*D+d])+bf2f(xn[t*D+d])*y+a);} }

typedef unsigned v4u __attribute__((ext_vector_type(4)));
__device__ __forceinline__ unsigned long long rt() { return __builtin_amdgcn_s_memrealtime(); }
struct PA { const char* p[7]; };
template<int MODE>
__global__ __launch_bounds__(512) void k_probe(PA a, unsigned* sink){
  unsigned long long t0=rt();
  int b=blockIdx.x; unsigned acc=0;
  if (MODE==0) {
    v4u r[7];
    #pragma unroll
    for (int i=0;i<4;++i) r[i]=*(const v4u*)(a.p[i]+b*8192+threadIdx.x*16);
    #pragma unroll
    for (int i=4;i<7;++i) r[i]=*(const v4u*)(a.p[i]+b*7168+(threadIdx.x%448)*16);
    #pragma unroll
    for (int i=0;i<7;++i) acc+=r[i].x^r[i].y^r[i].z^r[i].w;
  } else if (MODE==1) {
    v4u r=*(const v4u*)(a.p[0]+b*8192+(threadIdx.x&63)*16); acc+=r.x^r.y^r.z^r.w;
  } else if (MODE==2) {
    v4u r=*(const v4u*)(a.p[6]+b*7168+(threadIdx.x&63)*16); acc+=r.x^r.y^r.z^r.w;
  } else if (MODE==3) {
  }
  if (acc==0x12345678) sink[0]=acc;
  __syncthreads();
  unsigned long long t1=rt();
  unsigned long long d=t1-t0; if (d>2000) d=2000;
  while (rt()-t0 < 200*d) __builtin_amdgcn_s_sleep(8);
}
extern "C" void kernel_launch(void* const* d_in, const int* in_sizes, int n_in, void* d_out, int out_size, void* d_ws, size_t ws_size, hipStream_t stream) {
  const u16* q=(const u16*)d_in[0]; const u16* k=(const u16*)d_in[1]; const u16* v=(const u16*)d_in[2]; const u16* sk=(const u16*)d_in[3];
  const u16* ssm=(const u16*)d_in[4]; const u16* sd=(const u16*)d_in[5]; const u16* wg=(const u16*)d_in[7]; const u16* wu=(const u16*)d_in[8]; const u16* wd=(const u16*)d_in[9];
  char* ws=(char*)d_ws; u16* xn=(u16*)ws; u16* kvT=(u16*)(ws+(2<<20)); float* yT=(float*)(ws+(4<<20)); u16* h=(u16*)(ws+(8<<20));
  PA pa; pa.p[0]=(const char*)q; pa.p[1]=(const char*)k; pa.p[2]=(const char*)v; pa.p[3]=(const char*)ssm; pa.p[4]=(const char*)wg; pa.p[5]=(const char*)wu; pa.p[6]=(const char*)wd;
  k_probe<3><<<256,512,0,stream>>>(pa,(unsigned*)(ws+(32<<20)));
  k_prep<<<S,512,0,stream>>>(q,k,v,sk,xn,kvT);
  k_g1<<<S,256,0,stream>>>(xn,wg,wu,h);
  k_conv<<<D,256,0,stream>>>(kvT,ssm,yT);
  k_g2<<<S,256,0,stream>>>(q,xn,h,wd,kvT,yT,sd,(u16*)d_out);
}
```

```cpp
#include <hip/hip_runtime.h>
#include <hip/hip_bf16.h>
#include <stdint.h>
typedef unsigned short u16;
static __device__ __forceinline__ float bf2f(u16 v){ return __uint_as_float(((uint32_t)v)<<16); }
static __device__ __forceinline__ u16 f2bf(float f){ __hip_bfloat16 b = __float2bfloat16(f); return *(u16*)&b; }
static __device__ __forceinline__ float rbf(float f){ return bf2f(f2bf(f)); }
constexpr int S=2048, D=512, F=1792;
__global__ void k_prep(const u16* q,const u16* k,const u16* v,const u16* sk,u16* xn,u16* kvT){
  int t=blockIdx.x; int d=threadIdx.x; __shared__ float red[512];
  float x=bf2f(q[t*D+d]); red[d]=x; __syncthreads();
  for(int o=256;o>0;o>>=1){ if(d<o) red[d]+=red[d+o]; __syncthreads(); }
  float m=red[0]/D; __syncthreads(); red[d]=(x-m)*(x-m); __syncthreads();
  for(int o=256;o>0;o>>=1){ if(d<o) red[d]+=red[d+o]; __syncthreads(); }
  float var=red[0]/D;
  xn[t*D+d]=f2bf((x-m)/sqrtf(var+1e-5f));
  float ks=0; for(int j=0;j<4;j++){ int tt=t-3+j; float kk= tt>=0? bf2f(k[tt*D+d]):0.f; ks+= kk*bf2f(sk[d*4+3-j]); }
  kvT[d*S+t]=f2bf(rbf(ks)*bf2f(v[t*D+d]));
}
__global__ void k_g1(const u16* xn,const u16* wg,const u16* wu,u16* h){
  int t=blockIdx.x; __shared__ float xs[512]; for(int i=threadIdx.x;i<D;i+=256) xs[i]=bf2f(xn[t*D+i]); __syncthreads();
  for(int f=threadIdx.x;f<F;f+=256){ float g=0,u=0; for(int d=0;d<D;d++){ g+=xs[d]*bf2f(wg[d*F+f]); u+=xs[d]*bf2f(wu[d*F+f]);} h[t*F+f]=f2bf((g>0?g:0.f)*u);} }
__global__ void k_conv(const u16* kvT,const u16* ssm,float* yT){
  int d=blockIdx.x; __shared__ float us[S], hs[S]; for(int i=threadIdx.x;i<S;i+=256){us[i]=bf2f(kvT[d*S+i]); hs[i]=bf2f(ssm[d*S+i]);} __syncthreads();
  for(int t=threadIdx.x;t<S;t+=256){ float a=0; for(int s=0;s<=t;s++) a+=hs[t-s]*us[s]; yT[d*S+t]=a; } }
__global__ void k_g2(const u16* q,const u16* xn,const u16* h,const u16* wd,const u16* kvT,const float* yT,const u16* sd,u16* out){
  int t=blockIdx.x; __shared__ float hs[F]; for(int i=threadIdx.x;i<F;i+=256) hs[i]=bf2f(h[t*F+i]); __syncthreads();
  for(int d=threadIdx.x;d<D;d+=256){ float a=0; for(int f=0;f<F;f++) a+=hs[f]*bf2f(wd[f*D+d]);
    float y=yT[d*S+t]+bf2f(kvT[d*S+t])*bf2f(sd[d]); out[t*D+d]=f2bf(bf2f(q[t*D+d])+bf2f(xn[t*D+d])*y+a);} }

__device__ __forceinline__ unsigned long long rt() { return __builtin_amdgcn_s_memrealtime(); }
__device__ __forceinline__ unsigned long long ct() { return __builtin_amdgcn_s_memtime(); }
__global__ __launch_bounds__(512) void k_probe(unsigned* sink){
  unsigned long long t0=rt(); unsigned long long c0=ct();
  unsigned long long t1; do { t1=rt(); } while (t1-t0 < 500);
  unsigned long long c1=ct();
  unsigned long long mhz=(c1-c0)*100/(t1-t0);
  if (mhz>3000) mhz=3000;
  while (rt()-t0 < 48000) __builtin_amdgcn_s_sleep(8);
}
extern "C" void kernel_launch(void* const* d_in, const int* in_sizes, int n_in, void* d_out, int out_size, void* d_ws, size_t ws_size, hipStream_t stream) {
  const u16* q=(const u16*)d_in[0]; const u16* k=(const u16*)d_in[1]; const u16* v=(const u16*)d_in[2]; const u16* sk=(const u16*)d_in[3];
  const u16* ssm=(const u16*)d_in[4]; const u16* sd=(const u16*)d_in[5]; const u16* wg=(const u16*)d_in[7]; const u16* wu=(const u16*)d_in[8]; const u16* wd=(const u16*)d_in[9];
  char* ws=(char*)d_ws; u16* xn=(u16*)ws; u16* kvT=(u16*)(ws+(2<<20)); float* yT=(float*)(ws+(4<<20)); u16* h=(u16*)(ws+(8<<20));
  k_probe<<<256,64,0,stream>>>((unsigned*)(ws+(32<<20)));
  k_prep<<<S,512,0,stream>>>(q,k,v,sk,xn,kvT);
  k_g1<<<S,256,0,stream>>>(xn,wg,wu,h);
  k_conv<<<D,256,0,stream>>>(kvT,ssm,yT);
  k_g2<<<S,256,0,stream>>>(q,xn,h,wd,kvT,yT,sd,(u16*)d_out);
}
```

```cpp
#include <hip/hip_runtime.h>
#include <stdint.h>

typedef unsigned short u16;
typedef unsigned v2u __attribute__((ext_vector_type(2)));
typedef unsigned v4u __attribute__((ext_vector_type(4)));
typedef float v2f __attribute__((ext_vector_type(2)));
typedef float v4f __attribute__((ext_vector_type(4)));
typedef short v4s __attribute__((ext_vector_type(4)));
typedef __bf16 v2bf __attribute__((ext_vector_type(2)));
typedef __bf16 v8bf __attribute__((ext_vector_type(8)));
typedef __amdgpu_buffer_rsrc_t rsrc_t;
#define LAS __attribute__((address_space(3)))
#define DEV __device__ __forceinline__

constexpr unsigned WS_EPOCH = 0;
constexpr unsigned WS_FA = 256;
constexpr unsigned WS_FE = 1024;
constexpr unsigned WS_FH = 4096;
constexpr unsigned WS_MEAN = 16384;
constexpr unsigned WS_RSTD = 32768;
constexpr unsigned WS_CS = 49152;
constexpr unsigned WS_U = 131072;
constexpr unsigned WS_Z = WS_U + (2u << 20);
constexpr unsigned WS_H = WS_Z + (4u << 20);
constexpr unsigned WS_END = WS_H + 2048u * 1792u * 2u;
constexpr unsigned SLOT = 49152, L_SP = 147456, LDS_BYTES = 163840;
constexpr unsigned L_U = SLOT + 32768;
constexpr unsigned L_CARRY = L_U + 9216;
constexpr unsigned L_SCR = L_CARRY + 1088;
constexpr int SC1 = 16;
constexpr unsigned SPIN_CAP = 1u << 16;

struct Params {
  const u16 *q, *k, *v, *sk, *ssm, *sd, *wg, *wu, *wd;
  u16* out;
  char* ws;
};

DEV rsrc_t mk(const void* p, unsigned bytes) { return __builtin_amdgcn_make_buffer_rsrc((void*)p, (short)0, (int)bytes, 0x00020000); }
DEV void glds(unsigned voff, rsrc_t rs, unsigned soff, unsigned lds) {
  asm volatile("s_mov_b32 m0, %3\n\ts_nop 0\n\tbuffer_load_dwordx4 %0, %1, %2 offen lds" ::"v"(voff), "s"(rs), "s"(soff), "s"(lds) : "memory");
}
#define WAITV(n) asm volatile("s_waitcnt vmcnt(" #n ")" ::: "memory")
#define WAITL0() asm volatile("s_waitcnt lgkmcnt(0)" ::: "memory")
#define BAR() do { __builtin_amdgcn_sched_barrier(0); __builtin_amdgcn_s_barrier(); __builtin_amdgcn_sched_barrier(0); } while (0)
#define SB() __builtin_amdgcn_sched_barrier(0)
DEV v4u lds_r128(unsigned a) { return *(const LAS v4u*)(a); }
DEV v2u lds_r64(unsigned a) { return *(const LAS v2u*)(a); }
DEV unsigned lds_r32(unsigned a) { return *(const LAS unsigned*)(a); }
DEV void lds_w128(unsigned a, v4u v) { *(LAS v4u*)(a) = v; }
DEV void lds_w32(unsigned a, unsigned v) { *(LAS unsigned*)(a) = v; }
DEV v2u lds_tr(unsigned a) { return __builtin_bit_cast(v2u, __builtin_amdgcn_ds_read_tr16_b64_v4i16((LAS v4s*)(a))); }
DEV v4u lds_tr2(unsigned a0, unsigned a1) { v2u x = lds_tr(a0), y = lds_tr(a1); return v4u{x.x, x.y, y.x, y.y}; }
DEV v4f mfma(v4u a, v4u b, v4f c) { return __builtin_amdgcn_mfma_f32_16x16x32_bf16(__builtin_bit_cast(v8bf, a), __builtin_bit_cast(v8bf, b), c, 0, 0, 0); }
DEV float lo(unsigned d) { return __uint_as_float(d << 16); }
DEV float hi(unsigned d) { return __uint_as_float(d & 0xffff0000u); }
DEV unsigned pk(float a, float b) { v2bf r = {(__bf16)a, (__bf16)b}; return __builtin_bit_cast(unsigned, r); }
DEV float shr1(float x) { return __int_as_float(__builtin_amdgcn_update_dpp(0, __float_as_int(x), 0x138, 0xf, 0xf, true)); }
DEV float wsum(float x) {
#pragma unroll
  for (int o = 32; o > 0; o >>= 1) x += __shfl_xor(x, o);
  return x;
}
DEV unsigned ld32(rsrc_t r, unsigned off) { return __builtin_amdgcn_raw_buffer_load_b32(r, off, 0, SC1); }
DEV v2u ld64(rsrc_t r, unsigned off) { return __builtin_amdgcn_raw_buffer_load_b64(r, off, 0, SC1); }
DEV v4u ld128(rsrc_t r, unsigned off, unsigned soff) { return __builtin_amdgcn_raw_buffer_load_b128(r, off, soff, SC1); }
DEV v4u ld128p(rsrc_t r, unsigned off, unsigned soff) { return __builtin_amdgcn_raw_buffer_load_b128(r, off, soff, 0); }
DEV void st32(rsrc_t r, unsigned off, unsigned v) { __builtin_amdgcn_raw_buffer_store_b32(v, r, off, 0, SC1); }
DEV void st64(rsrc_t r, unsigned off, v2u v) { __builtin_amdgcn_raw_buffer_store_b64(v, r, off, 0, SC1); }
DEV void st128(rsrc_t r, unsigned off, unsigned soff, v4u v) { __builtin_amdgcn_raw_buffer_store_b128(v, r, off, soff, SC1); }
DEV void relax() { asm volatile("s_sleep 1" ::: "memory"); }

#define STAMP(i)

__global__ __launch_bounds__(512, 2) void fused_kernel(Params p) {
  const unsigned tid = threadIdx.x, l = tid & 63;
  const unsigned w = __builtin_amdgcn_readfirstlane(tid >> 6);
  const unsigned b = blockIdx.x, xcd = b & 7, r = b >> 3;
  const bool isG1 = r < 28;
  STAMP(0);
  const rsrc_t rq = mk(p.q, 2u << 20), rws = mk(p.ws, WS_END), rwd = mk(p.wd, 1792u * 512u * 2u), rssm = mk(p.ssm, 2u << 20);
  const rsrc_t rwg = mk(p.wg, 1792u * 512u * 2u), rwu = mk(p.wu, 1792u * 512u * 2u);
  const unsigned g = l >> 4, qq = (l >> 2) & 3, pp = l & 3;
  const unsigned rt = xcd * 4 + (r & 3), ct = r >> 2;
  const unsigned vod = (l >> 3) * 1024 + ((((l >> 1) & 3) ^ ((l >> 4) & 3)) << 5) + ((l & 1) << 4);
  const unsigned sod = w * 224 * 1024 + ct * 128;
  const unsigned ldA = 2 * SLOT + w * 6144, ldB = w * 6144, ldC = SLOT + w * 4096;
  const unsigned ssm_s = b * 8192 + w * 1024, ssm_l = L_SP + (w >> 2) * 4128 + 32 + (w & 3) * 1024;
  unsigned tag;
  v4u uv;
  if (tid < 4) lds_w128(L_SP + (tid >> 1) * 4128 + (tid & 1) * 16, v4u{0, 0, 0, 0});

  unsigned rt1 = 0, ks1 = 0;
  if (isG1) {
    const unsigned mt = (xcd >> 2) * 4 + (r & 3), nt = (xcd & 3) * 7 + (r >> 2);
    const unsigned koff = 2 * (xcd & 3) + 4 * (xcd >> 2);
    const unsigned mq = w >> 1, nh = w & 1;
    rt1 = mt * 4 + mq; ks1 = nt * 2 + nh;
    const unsigned vox = (8 * w + (l >> 3)) * 1024 + (((l & 7) ^ ((4 * (w & 1) + (l >> 4)) & 7)) << 4);
    const unsigned vow = (8 * w + (l >> 3)) * 3584 + ((((l >> 1) & 3) ^ (((l >> 4) & 1) | ((w & 1) << 1))) << 5) + ((l & 1) << 4);
    const unsigned sx0 = mt * 262144, sw0 = nt * 128, lw = w * 1024;
#define KST(s) (((s) + koff) & 7)
#define LD_X(s, slot, i) glds(vox, rq, sx0 + (i) * 65536 + KST(s) * 128, (slot) + lw + (i) * 8192)
#define LD_G(s, slot) glds(vow, rwg, sw0 + KST(s) * 229376, (slot) + lw + 32768)
#define LD_U(s, slot) glds(vow, rwu, sw0 + KST(s) * 229376, (slot) + lw + 40960)
    LD_X(0, 0, 0); LD_G(0, 0); LD_U(0, 0); LD_X(0, 0, 1); LD_X(0, 0, 2); LD_X(0, 0, 3);
    LD_X(1, SLOT, 0); LD_G(1, SLOT); LD_U(1, SLOT); LD_X(1, SLOT, 1); LD_X(1, SLOT, 2); LD_X(1, SLOT, 3);
    LD_X(2, 2 * SLOT, 0); LD_X(2, 2 * SLOT, 1); LD_G(2, 2 * SLOT);
    const unsigned ax0 = (64 * mq + (l & 15)) * 128 + ((g ^ ((l & 15) >> 1)) << 4), ax1 = ax0 ^ 64;
    const unsigned aw0 = 32768 + (8 * g + qq) * 128 + (((2 * nh) ^ ((qq >> 1) | ((g & 1) << 1))) << 5) + pp * 8, aw1 = aw0 ^ 32;
    v4f acc[4][4];
#pragma unroll
    for (int i = 0; i < 4; ++i)
#pragma unroll
      for (int j = 0; j < 4; ++j) acc[i][j] = v4f{0, 0, 0, 0};
    v4u X0[4], W0[4], X1[4], W1[4];
#define RD_X(dst, i, base) dst[i] = lds_r128((base) + (i) * 2048)
#define RD_W(dst, t, b0, b1, ks) dst[t] = lds_tr2(((t) & 1 ? (b1) : (b0)) + ((t) >> 1) * 8192 + (ks) * 4096, ((t) & 1 ? (b1) : (b0)) + ((t) >> 1) * 8192 + (ks) * 4096 + 512)
#define MM(X, W, i, j) acc[i][j] = mfma(W[j], X[i], acc[i][j])
#define HALF(X, W, XN, WN, bx, b0, b1, ks, RD, L0, L1, L2) do { \
      MM(X, W, 0, 0); if (RD) RD_W(WN, 0, b0, b1, ks); SB(); \
      MM(X, W, 0, 1); if (RD) RD_X(XN, 0, bx); SB(); \
      MM(X, W, 0, 2); if (RD) RD_W(WN, 1, b0, b1, ks); SB(); \
      MM(X, W, 0, 3); L0; SB(); \
      MM(X, W, 1, 0); if (RD) RD_W(WN, 2, b0, b1, ks); SB(); \
      MM(X, W, 1, 1); if (RD) RD_X(XN, 1, bx); SB(); \
      MM(X, W, 1, 2); if (RD) RD_W(WN, 3, b0, b1, ks); SB(); \
      MM(X, W, 1, 3); L1; SB(); \
      MM(X, W, 2, 0); if (RD) RD_X(XN, 2, bx); SB(); \
      MM(X, W, 2, 1); if (RD) RD_X(XN, 3, bx); SB(); \
      MM(X, W, 2, 2); SB(); \
      MM(X, W, 2, 3); L2; SB(); \
      MM(X, W, 3, 0); MM(X, W, 3, 1); MM(X, W, 3, 2); MM(X, W, 3, 3); SB(); } while (0)
    WAITV(9); BAR();
    STAMP(1);
#pragma unroll
    for (int i = 0; i < 4; ++i) { RD_W(W0, i, aw0, aw1, 0); RD_X(X0, i, ax0); }
    SB();
    unsigned cur = 0, nxt = SLOT, prv = 2 * SLOT;
    for (unsigned s = 0; s < 5; ++s) {
      HALF(X0, W0, X1, W1, ax1 + cur, aw0 + cur, aw1 + cur, 1, 1, LD_X(s + 2, prv, 2), LD_X(s + 2, prv, 3), LD_U(s + 2, prv));
      WAITV(6); WAITL0(); BAR();
      HALF(X1, W1, X0, W0, ax0 + nxt, aw0 + nxt, aw1 + nxt, 0, 1, LD_X(s + 3, cur, 0), LD_X(s + 3, cur, 1), LD_G(s + 3, cur));
      unsigned t = cur; cur = nxt; nxt = prv; prv = t;
    }
    STAMP(2);
#define LD_D(pc, lds) glds(vod, rwd, sod + (pc) * 8192, lds)
    HALF(X0, W0, X1, W1, ax1 + 2 * SLOT, aw0 + 2 * SLOT, aw1 + 2 * SLOT, 1, 1, LD_X(7, SLOT, 2), LD_X(7, SLOT, 3), LD_U(7, SLOT));
    WAITV(6); WAITL0(); BAR();
    glds(l * 16, rssm, ssm_s, ssm_l);
    HALF(X1, W1, X0, W0, ax0, aw0, aw1, 0, 1, LD_D(0, ldA), LD_D(1, ldA + 1024), LD_D(2, ldA + 2048));
    HALF(X0, W0, X1, W1, ax1, aw0, aw1, 1, 1, LD_D(3, ldA + 3072), LD_D(4, ldA + 4096), LD_D(5, ldA + 5120));
    WAITV(7); WAITL0(); BAR();
    const unsigned pofs = l < 32 ? WS_FA + l * 4 : WS_EPOCH;
    unsigned p1;
    asm volatile("buffer_load_dword %0, %1, %2, 0 offen sc1" : "=v"(p1) : "v"(pofs), "s"(rws) : "memory");
    HALF(X1, W1, X0, W0, ax0 + SLOT, aw0 + SLOT, aw1 + SLOT, 0, 1, LD_D(6, ldB), LD_D(7, ldB + 1024), LD_D(8, ldB + 2048));
    HALF(X0, W0, X1, W1, ax1 + SLOT, aw0 + SLOT, aw1 + SLOT, 1, 1, LD_D(9, ldB + 3072), LD_D(10, ldB + 4096), LD_D(11, ldB + 5120));
    asm volatile("s_waitcnt vmcnt(6)" : "+v"(p1)::"memory");
    WAITL0(); BAR();
    tag = __builtin_amdgcn_readlane(p1, 32) + 1;
    if (tid == 0) st32(rws, WS_FE + b * 4, tag);
    for (unsigned sp = 0; !__all(l >= 32 || p1 == tag) && sp < SPIN_CAP; ++sp) { relax(); p1 = ld32(rws, pofs); }
    const unsigned om = WS_MEAN + (rt1 * 64 + l) * 8, oc = WS_CS + nt * 1024 + (l >> 5) * 512 + (nh * 32 + (l & 31)) * 8;
    v2u gm, gc;
    asm volatile("buffer_load_dwordx2 %0, %1, %2, 0 offen sc1" : "=v"(gm) : "v"(om), "s"(rws) : "memory");
    asm volatile("buffer_load_dwordx2 %0, %1, %2, 0 offen sc1" : "=v"(gc) : "v"(oc), "s"(rws) : "memory");
    const unsigned ou = WS_U + b * 8192 + tid * 16;
    asm volatile("buffer_load_dwordx4 %0, %1, %2, 0 offen sc1" : "=v"(uv) : "v"(ou), "s"(rws) : "memory");
    LD_D(12, ldC);
    HALF(X1, W1, X0, W0, 0, 0, 0, 0, 0, LD_D(13, ldC + 1024), LD_D(14, ldC + 2048), LD_D(15, ldC + 3072));
    STAMP(3);
    asm volatile("s_waitcnt vmcnt(4)" : "+v"(gm), "+v"(gc), "+v"(uv)::"memory");
    for (unsigned sp = 0; !__all(gm.y == tag && gc.y == tag) && sp < SPIN_CAP; ++sp) { relax(); gm = ld64(rws, om); gc = ld64(rws, oc); }
    STAMP(4);
    const unsigned scr = L_SCR + w * 512;
    lds_w32(scr + l * 4, gm.x);
    lds_w32(scr + 256 + l * 4, gc.x);
    v4f cs[4];
#pragma unroll
    for (int j = 0; j < 4; ++j) cs[j] = __builtin_bit_cast(v4f, lds_r128(scr + 256 + j * 64 + g * 16));
#pragma unroll
    for (int i = 0; i < 4; ++i) {
      const float m = -__uint_as_float(lds_r32(scr + i * 64 + (l & 15) * 4));
      float h[8];
#pragma unroll
      for (int e = 0; e < 8; ++e) {
        const float gt = fmaf(m, cs[e >> 2][e & 3], acc[i][e >> 2][e & 3]);
        const float up = fmaf(m, cs[2 + (e >> 2)][e & 3], acc[i][2 + (e >> 2)][e & 3]);
        h[e] = fmaxf(gt, 0.f) * up;
      }
      st128(rws, l * 16, WS_H + ((rt1 * 56 + ks1) * 4 + i) * 1024, v4u{pk(h[0], h[1]), pk(h[2], h[3]), pk(h[4], h[5]), pk(h[6], h[7])});
    }
    SB();
  } else {
    const unsigned al = r - 28, a = xcd * 4 + al;
    const rsrc_t rk = mk(p.k, 2u << 20), rv = mk(p.v, 2u << 20), rsk = mk(p.sk, 4096);
    {
      unsigned long long pc;
      asm volatile("s_getpc_b64 %0" : "=s"(pc));
      (void)pc;
    }
    tag = __builtin_amdgcn_readfirstlane(ld32(rws, WS_EPOCH)) + 1;
    if (tid == 0) st32(rws, WS_FE + b * 4, tag);
    v4u kk[11], vv[8], sk[4];
    const int t0 = a * 64 + w * 8;
#pragma unroll
    for (int i = 0; i < 4; ++i) sk[i] = ld128p(rsk, l * 64 + i * 16, 0);
#pragma unroll
    for (int i = 0; i < 11; ++i) {
      const int t = t0 - 3 + i;
      kk[i] = ld128p(rk, l * 16 + (t < 0 ? 0x7ff00000u : (unsigned)t * 1024), 0);
    }
#pragma unroll
    for (int i = 0; i < 8; ++i) vv[i] = ld128p(rv, l * 16, (t0 + i) * 1024);
    SB();
    const unsigned cidx = (xcd >> 2) * 4 + al;
    const unsigned cnt = (xcd & 3) * 7 + (cidx < 7 ? cidx : 0);
    const rsrc_t rwc = (w >> 2) ? rwu : rwg;
    const unsigned cvo = (l >> 3) * 3584 + (l & 7) * 16, cso = (w & 3) * 128 * 3584 + cnt * 128;
    v4u cw[16];
#pragma unroll
    for (int j = 0; j < 4; ++j) {
#pragma unroll
      for (int c = 0; c < 2; ++c) {
        const unsigned s01 = sk[j][2 * c], s23 = sk[j][2 * c + 1];
        const float s0 = lo(s01), s1 = hi(s01), s2 = lo(s23), s3 = hi(s23);
        float kf[11], o[8];
#pragma unroll
        for (int i = 0; i < 11; ++i) kf[i] = c ? hi(kk[i][j]) : lo(kk[i][j]);
#pragma unroll
        for (int i = 0; i < 8; ++i) {
          const float ks = fmaf(kf[i + 3], s0, fmaf(kf[i + 2], s1, fmaf(kf[i + 1], s2, kf[i] * s3)));
          o[i] = ks * (c ? hi(vv[i][j]) : lo(vv[i][j]));
        }
        lds_w128((8 * l + 2 * j + c) * 128 + ((w ^ (l & 7)) << 4), v4u{pk(o[0], o[1]), pk(o[2], o[3]), pk(o[4], o[5]), pk(o[6], o[7])});
      }
      SB();
#pragma unroll
      for (int i = 0; i < 4; ++i) cw[4 * j + i] = ld128p(rwc, cvo, cso + (4 * j + i) * 8 * 3584);
      SB();
    }
    STAMP(1);
    WAITL0(); BAR();
#pragma unroll
    for (int i = 0; i < 8; ++i) {
      const unsigned ch = 64 * w + 8 * i + (l >> 3);
      st128(rws, ch * 4096 + (l & 7) * 16, WS_U + a * 128, lds_r128(ch * 128 + (((l & 7) ^ i) << 4)));
    }
    SB();
    v4u xr[8];
#pragma unroll
    for (int i = 0; i < 8; ++i) xr[i] = ld128p(rq, l * 16, (t0 + i) * 1024);
    SB();
    float cs[8];
#pragma unroll
    for (int e = 0; e < 8; ++e) cs[e] = 0.f;
#pragma unroll
    for (int i = 0; i < 16; ++i)
#pragma unroll
      for (int j = 0; j < 4; ++j) { cs[2 * j] += lo(cw[i][j]); cs[2 * j + 1] += hi(cw[i][j]); }
#pragma unroll
    for (int e = 0; e < 8; ++e) { cs[e] += __shfl_xor(cs[e], 8); cs[e] += __shfl_xor(cs[e], 16); cs[e] += __shfl_xor(cs[e], 32); }
    if (l < 8) {
      lds_w128(65536 + w * 256 + l * 32, __builtin_bit_cast(v4u, v4f{cs[0], cs[1], cs[2], cs[3]}));
      lds_w128(65536 + w * 256 + l * 32 + 16, __builtin_bit_cast(v4u, v4f{cs[4], cs[5], cs[6], cs[7]}));
    }
    WAITV(8); WAITL0(); BAR();
    STAMP(2);
    if (tid == 0) st32(rws, WS_FA + a * 4, tag);
    if (tid < 128 && cidx < 7) {
      const unsigned ab = 65536 + (tid >> 6) * 1024 + l * 4;
      const float s = (__uint_as_float(lds_r32(ab)) + __uint_as_float(lds_r32(ab + 256))) + (__uint_as_float(lds_r32(ab + 512)) + __uint_as_float(lds_r32(ab + 768)));
      st64(rws, WS_CS + cnt * 1024 + tid * 8, v2u{__float_as_uint(s), tag});
    }
    float mean[8], mysel = 0.f;
#pragma unroll
    for (int i = 0; i < 8; ++i) {
      float s = 0.f;
#pragma unroll
      for (int j = 0; j < 4; ++j) s += lo(xr[i][j]) + hi(xr[i][j]);
      mean[i] = wsum(s) * (1.f / 512.f);
      mysel = l == (unsigned)i ? mean[i] : mysel;
    }
    if (l < 8) st64(rws, WS_MEAN + (t0 + l) * 8, v2u{__float_as_uint(mysel), tag});
    STAMP(3);
    WAITL0(); BAR();
    glds(l * 16, rssm, ssm_s, ssm_l);
#pragma unroll
    for (int i = 0; i < 6; ++i) glds(vod, rwd, sod + i * 8192, ldA + i * 1024);
#pragma unroll
    for (int i = 0; i < 8; ++i) {
      float s = 0.f;
#pragma unroll
      for (int j = 0; j < 4; ++j) { const float d0 = lo(xr[i][j]) - mean[i], d1 = hi(xr[i][j]) - mean[i]; s = fmaf(d0, d0, fmaf(d1, d1, s)); }
      const float rs = 1.f / sqrtf(wsum(s) * (1.f / 512.f) + 1e-5f);
      mysel = l == (unsigned)i ? rs : mysel;
    }
    if (l < 8) st64(rws, WS_RSTD + (t0 + l) * 8, v2u{__float_as_uint(mysel), tag});
#pragma unroll
    for (int i = 0; i < 6; ++i) glds(vod, rwd, sod + (6 + i) * 8192, ldB + i * 1024);
#pragma unroll
    for (int i = 0; i < 4; ++i) glds(vod, rwd, sod + (12 + i) * 8192, ldC + i * 1024);
    unsigned p1 = ld32(rws, WS_FA + (l & 31) * 4);
    for (unsigned sp = 0; !__all(p1 == tag) && sp < SPIN_CAP; ++sp) { relax(); p1 = ld32(rws, WS_FA + (l & 31) * 4); }
    uv = ld128(rws, tid * 16, WS_U + b * 8192);
    asm volatile("s_waitcnt vmcnt(0)" : "+v"(uv)::"memory");
    BAR();
    STAMP(4);
  }
  STAMP(5);
  lds_w128(L_U + (tid >> 8) * 4576 + 480 + (tid & 255) * 16, uv);
  if (tid < 62) lds_w128(tid < 60 ? L_U + (tid >= 30 ? 4576 - 480 : 0) + tid * 16 : L_CARRY + (tid - 60) * 528, v4u{0, 0, 0, 0});
  v4u WD[7][4];
  const unsigned adr = (4 * g + qq) * 128 + pp * 8, afx = ((2 * g + (qq >> 1)) & 3) << 5;
#define PIECE(pc) ((pc) < 6 ? ldA + (pc) * 1024 : (pc) < 12 ? ldB + ((pc) - 6) * 1024 : (pc) < 16 ? ldC + ((pc) - 12) * 1024 : (pc) < 22 ? ldA + ((pc) - 16) * 1024 : ldB + ((pc) - 22) * 1024)
#define RD_D(ks) _Pragma("unroll") for (int n = 0; n < 4; ++n) WD[ks][n] = lds_tr2(PIECE(4 * (ks)) + adr + ((n << 5) ^ afx), PIECE(4 * (ks) + 2) + adr + ((n << 5) ^ afx))
  RD_D(0); RD_D(1); RD_D(2);
  const unsigned ch = w >> 2, c = w & 3;
  v4u H[16];
  const unsigned aH = L_SP + ch * 4128 + 224 + 8 * pp - 64 * g - 8 * qq;
#pragma unroll
  for (int z = 0; z < 16; ++z) H[z] = lds_tr2(aH + 256 * z + 32, aH + 256 * z);
  SB(); WAITL0(); SB();
#pragma unroll
  for (int i = 16; i < 28; ++i) glds(vod, rwd, sod + i * 8192, PIECE(i));
  BAR();
  STAMP(6);
  v4f ya[8];
#pragma unroll
  for (int i = 0; i < 8; ++i) ya[i] = v4f{0, 0, 0, 0};
  const unsigned aU = L_U + ch * 4576 + 128 * c + 32 * pp + 64 * g + 8 * qq;
  const unsigned aY = L_SP + ch * 8192 + c * 256 + l * 4;
  const unsigned oh = WS_FH + rt * 256 + w * 32 + (l < 7 ? l : 0) * 4;
  unsigned p2;
  {
    v4u Ua = lds_tr2(aU, aU + 32), Ub = lds_tr2(aU + 256, aU + 256 + 32);
#pragma unroll
    for (int j = 0; j < 16; ++j) {
      const v4u Uc = Ua;
      Ua = Ub;
      if (j + 2 < 16) Ub = lds_tr2(aU + 256 * (j + 2), aU + 256 * (j + 2) + 32);
#pragma unroll
      for (int v = j >> 1; v < 8; ++v) ya[v] = mfma(Uc, H[2 * v + 1 - j], ya[v]);
      if (j >= 2 && !(j & 1)) {
        const int v = (j >> 1) - 1;
        ya[v][2] += shr1(ya[v][3]); ya[v][1] += shr1(ya[v][2]); ya[v][0] += shr1(ya[v][1]);
        lds_w32(aY + v * 1024, __float_as_uint(ya[v][0]));
      }
      SB();
      if (j == 3) {
        WAITV(12);
        if (isG1 && l == 0) st32(rws, WS_FH + rt1 * 256 + (ks1 / 7) * 32 + (ks1 % 7) * 4, tag);
        SB();
      }
      if (j == 11) { asm volatile("buffer_load_dword %0, %1, %2, 0 offen sc1" : "=v"(p2) : "v"(oh), "s"(rws) : "memory"); SB(); }
    }
    ya[7][2] += shr1(ya[7][3]); ya[7][1] += shr1(ya[7][2]); ya[7][0] += shr1(ya[7][1]);
    lds_w32(aY + 7 * 1024, __float_as_uint(ya[7][0]));
    if (l == 63) {
#pragma unroll
      for (int v = 0; v < 8; ++v) lds_w128(L_CARRY + ch * 528 + (4 * v + c + 1) * 16, __builtin_bit_cast(v4u, v4f{ya[v][1], ya[v][2], ya[v][3], 0.f}));
    }
  }
  STAMP(7);
  asm volatile("s_waitcnt vmcnt(0)" : "+v"(p2)::"memory");
  for (unsigned sp = 0; !__all(p2 == tag) && sp < SPIN_CAP; ++sp) { relax(); p2 = ld32(rws, oh); }
  STAMP(8);
  const unsigned rot = (ct & 1) * 2;
  const unsigned sh = WS_H + (rt * 56 + w * 7) * 4096;
  v4u Ha[7], Hb[7];
#define LD_H(dst, mi) _Pragma("unroll") for (int ks = 0; ks < 7; ++ks) dst[ks] = ld128(rws, l * 16 + ks * 4096, sh + (((mi) ^ rot) << 10))
  LD_H(Ha, 0);
  SB();
  RD_D(3); RD_D(4); RD_D(5); RD_D(6);
  SB(); WAITL0(); BAR();
  {
    const unsigned sdp = *(const unsigned*)(p.sd + 2 * b);
    const float d0 = lo(sdp), d1 = hi(sdp);
#pragma unroll
    for (int i = 0; i < 2; ++i) {
      const unsigned pi = tid + 512 * i;
      const unsigned ca = L_CARRY + ((pi & 31) < 2 ? (pi >> 5) * 16 + (pi & 31) * 8 : 0);
      const v2f y0 = __builtin_bit_cast(v2f, lds_r64(L_SP + pi * 8)) + __builtin_bit_cast(v2f, lds_r64(ca));
      const v2f y1 = __builtin_bit_cast(v2f, lds_r64(L_SP + 8192 + pi * 8)) + __builtin_bit_cast(v2f, lds_r64(ca + 528));
      const unsigned u0 = lds_r32(L_U + 480 + pi * 4), u1 = lds_r32(L_U + 4576 + 480 + pi * 4);
      st128(rws, pi * 16, WS_Z + b * 16384, v4u{pk(fmaf(lo(u0), d0, y0.x), fmaf(lo(u1), d1, y1.x)), tag, pk(fmaf(hi(u0), d0, y0.y), fmaf(hi(u1), d1, y1.y)), tag});
    }
  }
  SB();
  STAMP(9);
  LD_H(Hb, 1);
  SB();
#define REDB(wv, mi, n) ((mi) == 0 ? 2 * SLOT + (wv) * 6144 + (n) * 1024 : (mi) == 1 ? (wv) * 6144 + (n) * 1024 : (mi) == 2 ? SLOT + (wv) * 4096 + (n) * 1024 \
                         : ((n) < 2 ? 2 * SLOT : 0) + (wv) * 6144 + 4096 + ((n) & 1) * 1024)
#define G2(Hx, mi) do { v4f o[4]; \
    _Pragma("unroll") for (int n = 0; n < 4; ++n) o[n] = v4f{0, 0, 0, 0}; \
    _Pragma("unroll") for (int ks = 0; ks < 7; ++ks) _Pragma("unroll") for (int n = 0; n < 4; ++n) o[n] = mfma(WD[ks][n], Hx[ks], o[n]); \
    _Pragma("unroll") for (int n = 0; n < 4; ++n) lds_w128(REDB(w, mi, n) + ((l ^ (((2 * n + (g >> 1)) & 3) << 2)) << 4), __builtin_bit_cast(v4u, o[n])); \
    SB(); } while (0)
  G2(Ha, 0);
  STAMP(10);
  LD_H(Ha, 2); SB();
  G2(Hb, 1);
  LD_H(Hb, 3); SB();
  const unsigned mloc = w >> 1;
  const unsigned row = rt * 64 + ((mloc ^ rot) << 4) + (w & 1) * 8 + (l >> 3), cg = l & 7;
  const v4u xv = ld128p(rq, row * 1024 + cg * 16, ct * 128);
  const unsigned oz = WS_Z + (ct * 32 + w * 4 + (l >> 5)) * 16384 + rt * 512 + (l & 31) * 16;
  v4u z0 = ld128(rws, oz, 0), z1 = ld128(rws, oz, 32768);
  v2u gm = ld64(rws, WS_MEAN + row * 8), gr = ld64(rws, WS_RSTD + row * 8);
  v4u fe = v4u{tag, tag, tag, tag};
  if (b == 0 && w == 7) fe = ld128(rws, WS_FE + l * 16, 0);
  SB();
  G2(Ha, 2);
  G2(Hb, 3);
  STAMP(11);
  for (unsigned sp = 0; !__all(z0.y == tag && z0.w == tag && z1.y == tag && z1.w == tag && gm.y == tag && gr.y == tag) && sp < SPIN_CAP; ++sp) {
    relax();
    z0 = ld128(rws, oz, 0); z1 = ld128(rws, oz, 32768); gm = ld64(rws, WS_MEAN + row * 8); gr = ld64(rws, WS_RSTD + row * 8);
  }
  {
    BAR();
    const unsigned za = L_U + ((l & 31) * 2 * 33 + w * 4 + (l >> 5)) * 4;
    lds_w32(za, z0.x); lds_w32(za + 132, z0.z); lds_w32(za + 8, z1.x); lds_w32(za + 140, z1.z);
  }
  WAITL0(); BAR();
  {
    const unsigned tok = (w & 1) * 8 + (l >> 3), n = cg >> 1, g0 = (cg & 1) * 2;
    const unsigned o0 = ((g0 * 16 + tok) ^ ((cg & 3) << 2)) << 4;
    const unsigned base = (mloc == 0 ? 2 * SLOT + n * 1024 : mloc == 1 ? n * 1024 : mloc == 2 ? SLOT + n * 1024 : (n < 2 ? 2 * SLOT : 0) + 4096 + (n & 1) * 1024) + o0;
    const unsigned str = mloc == 2 ? 4096 : 6144;
    v4f s0 = v4f{0, 0, 0, 0}, s1 = v4f{0, 0, 0, 0};
#pragma unroll
    for (int i = 0; i < 8; ++i) {
      s0 += __builtin_bit_cast(v4f, lds_r128(base + i * str));
      s1 += __builtin_bit_cast(v4f, lds_r128(base + i * str + 256));
    }
    const unsigned zr = L_U + ((row & 63) * 33 + cg * 4) * 4;
    const float mean = __uint_as_float(gm.x), rstd = __uint_as_float(gr.x), r2 = rstd * rstd;
    v4u o;
#pragma unroll
    for (int j = 0; j < 4; ++j) {
      const unsigned zz = lds_r32(zr + j * 4);
      const float x0 = lo(xv[j]), x1 = hi(xv[j]);
      const float b0 = j < 2 ? s0[2 * j] : s1[2 * j - 4], b1 = j < 2 ? s0[2 * j + 1] : s1[2 * j - 3];
      o[j] = pk(fmaf(r2, b0, fmaf((x0 - mean) * rstd, lo(zz), x0)), fmaf(r2, b1, fmaf((x1 - mean) * rstd, hi(zz), x1)));
    }
    *(v4u*)((char*)p.out + row * 1024 + ct * 128 + cg * 16) = o;
  }
  if (b == 0 && w == 7) {
    for (unsigned sp = 0; !__all(fe.x == tag && fe.y == tag && fe.z == tag && fe.w == tag) && sp < SPIN_CAP; ++sp) { relax(); fe = ld128(rws, WS_FE + l * 16, 0); }
    if (l == 0) st32(rws, WS_EPOCH, tag);
  }
  STAMP(12);
}

extern "C" void kernel_launch(void* const* d_in, const int* in_sizes, int n_in, void* d_out, int out_size, void* d_ws, size_t ws_size,
                              hipStream_t stream) {
  Params p;
  p.q = (const u16*)d_in[0]; p.k = (const u16*)d_in[1]; p.v = (const u16*)d_in[2]; p.sk = (const u16*)d_in[3];
  p.ssm = (const u16*)d_in[4]; p.sd = (const u16*)d_in[5]; p.wg = (const u16*)d_in[7]; p.wu = (const u16*)d_in[8];
  p.wd = (const u16*)d_in[9]; p.out = (u16*)d_out; p.ws = (char*)d_ws;
  (void)hipFuncSetAttribute((const void*)fused_kernel, hipFuncAttributeMaxDynamicSharedMemorySize, LDS_BYTES);
  fused_kernel<<<256, 512, LDS_BYTES, stream>>>(p);
}
```

```cpp
#define STAMPS
#include <hip/hip_runtime.h>
#include <stdint.h>

typedef unsigned short u16;
typedef unsigned v2u __attribute__((ext_vector_type(2)));
typedef unsigned v4u __attribute__((ext_vector_type(4)));
typedef float v2f __attribute__((ext_vector_type(2)));
typedef float v4f __attribute__((ext_vector_type(4)));
typedef short v4s __attribute__((ext_vector_type(4)));
typedef __bf16 v2bf __attribute__((ext_vector_type(2)));
typedef __bf16 v8bf __attribute__((ext_vector_type(8)));
typedef __amdgpu_buffer_rsrc_t rsrc_t;
#define LAS __attribute__((address_space(3)))
#define DEV __device__ __forceinline__

constexpr unsigned WS_EPOCH = 0;
constexpr unsigned WS_FA = 256;
constexpr unsigned WS_FE = 1024;
constexpr unsigned WS_FH = 4096;
constexpr unsigned WS_MEAN = 16384;
constexpr unsigned WS_RSTD = 32768;
constexpr unsigned WS_CS = 49152;
constexpr unsigned WS_U = 131072;
constexpr unsigned WS_Z = WS_U + (2u << 20);
constexpr unsigned WS_H = WS_Z + (4u << 20);
constexpr unsigned WS_END = WS_H + 2048u * 1792u * 2u;
constexpr unsigned WS_ST = 32u << 20;
constexpr int NST = 16;
constexpr unsigned SLOT = 49152, L_SP = 147456, LDS_BYTES = 163840;
constexpr unsigned L_U = SLOT + 32768;
constexpr unsigned L_CARRY = L_U + 9216;
constexpr unsigned L_SCR = L_CARRY + 1088;
constexpr int SC1 = 16;
constexpr unsigned SPIN_CAP = 1u << 16;

struct Params {
  const u16 *q, *k, *v, *sk, *ssm, *sd, *wg, *wu, *wd;
  u16* out;
  char* ws;
};

DEV rsrc_t mk(const void* p, unsigned bytes) { return __builtin_amdgcn_make_buffer_rsrc((void*)p, (short)0, (int)bytes, 0x00020000); }
DEV void glds(unsigned voff, rsrc_t rs, unsigned soff, unsigned lds) {
  asm volatile("s_mov_b32 m0, %3\n\ts_nop 0\n\tbuffer_load_dwordx4 %0, %1, %2 offen lds" ::"v"(voff), "s"(rs), "s"(soff), "s"(lds) : "memory");
}
#define WAITV(n) asm volatile("s_waitcnt vmcnt(" #n ")" ::: "memory")
#define WAITL0() asm volatile("s_waitcnt lgkmcnt(0)" ::: "memory")
#define BAR() do { __builtin_amdgcn_sched_barrier(0); __builtin_amdgcn_s_barrier(); __builtin_amdgcn_sched_barrier(0); } while (0)
#define SB() __builtin_amdgcn_sched_barrier(0)
DEV v4u lds_r128(unsigned a) { return *(const LAS v4u*)(a); }
DEV v2u lds_r64(unsigned a) { return *(const LAS v2u*)(a); }
DEV unsigned lds_r32(unsigned a) { return *(const LAS unsigned*)(a); }
DEV void lds_w128(unsigned a, v4u v) { *(LAS v4u*)(a) = v; }
DEV void lds_w32(unsigned a, unsigned v) { *(LAS unsigned*)(a) = v; }
DEV v2u lds_tr(unsigned a) { return __builtin_bit_cast(v2u, __builtin_amdgcn_ds_read_tr16_b64_v4i16((LAS v4s*)(a))); }
DEV v4u lds_tr2(unsigned a0, unsigned a1) { v2u x = lds_tr(a0), y = lds_tr(a1); return v4u{x.x, x.y, y.x, y.y}; }
DEV v4f mfma(v4u a, v4u b, v4f c) { return __builtin_amdgcn_mfma_f32_16x16x32_bf16(__builtin_bit_cast(v8bf, a), __builtin_bit_cast(v8bf, b), c, 0, 0, 0); }
DEV float lo(unsigned d) { return __uint_as_float(d << 16); }
DEV float hi(unsigned d) { return __uint_as_float(d & 0xffff0000u); }
DEV unsigned pk(float a, float b) { v2bf r = {(__bf16)a, (__bf16)b}; return __builtin_bit_cast(unsigned, r); }
DEV float shr1(float x) { return __int_as_float(__builtin_amdgcn_update_dpp(0, __float_as_int(x), 0x138, 0xf, 0xf, true)); }
DEV float wsum(float x) {
#pragma unroll
  for (int o = 32; o > 0; o >>= 1) x += __shfl_xor(x, o);
  return x;
}
DEV unsigned ld32(rsrc_t r, unsigned off) { return __builtin_amdgcn_raw_buffer_load_b32(r, off, 0, SC1); }
DEV v2u ld64(rsrc_t r, unsigned off) { return __builtin_amdgcn_raw_buffer_load_b64(r, off, 0, SC1); }
DEV v4u ld128(rsrc_t r, unsigned off, unsigned soff) { return __builtin_amdgcn_raw_buffer_load_b128(r, off, soff, SC1); }
DEV v4u ld128p(rsrc_t r, unsigned off, unsigned soff) { return __builtin_amdgcn_raw_buffer_load_b128(r, off, soff, 0); }
DEV void st32(rsrc_t r, unsigned off, unsigned v) { __builtin_amdgcn_raw_buffer_store_b32(v, r, off, 0, SC1); }
DEV void st64(rsrc_t r, unsigned off, v2u v) { __builtin_amdgcn_raw_buffer_store_b64(v, r, off, 0, SC1); }
DEV void st128(rsrc_t r, unsigned off, unsigned soff, v4u v) { __builtin_amdgcn_raw_buffer_store_b128(v, r, off, soff, SC1); }
DEV void relax() { asm volatile("s_sleep 1" ::: "memory"); }

#define STAMP(i) do { SB(); unsigned long long t_; asm volatile("s_memrealtime %0\n\ts_waitcnt lgkmcnt(0)" : "=s"(t_) :: "memory"); T[i] = (unsigned)t_; SB(); } while (0)

__global__ __launch_bounds__(512, 2) void fused_kernel(Params p) {
  const unsigned tid = threadIdx.x, l = tid & 63;
  const unsigned w = __builtin_amdgcn_readfirstlane(tid >> 6);
  const unsigned b = blockIdx.x, xcd = b & 7, r = b >> 3;
  const bool isG1 = r < 28;
  unsigned T[NST];
#pragma unroll
  for (int i = 0; i < NST; ++i) T[i] = 0;
  STAMP(0);
  const rsrc_t rq = mk(p.q, 2u << 20), rws = mk(p.ws, WS_END), rwd = mk(p.wd, 1792u * 512u * 2u), rssm = mk(p.ssm, 2u << 20);
  const rsrc_t rwg = mk(p.wg, 1792u * 512u * 2u), rwu = mk(p.wu, 1792u * 512u * 2u);
  const unsigned g = l >> 4, qq = (l >> 2) & 3, pp = l & 3;
  const unsigned rt = xcd * 4 + (r & 3), ct = r >> 2;
  const unsigned vod = (l >> 3) * 1024 + ((((l >> 1) & 3) ^ ((l >> 4) & 3)) << 5) + ((l & 1) << 4);
  const unsigned sod = w * 224 * 1024 + ct * 128;
  const unsigned ldA = 2 * SLOT + w * 6144, ldB = w * 6144, ldC = SLOT + w * 4096;
  const unsigned ssm_s = b * 8192 + w * 1024, ssm_l = L_SP + (w >> 2) * 4128 + 32 + (w & 3) * 1024;
  unsigned tag;
  v4u uv;
  if (tid < 4) lds_w128(L_SP + (tid >> 1) * 4128 + (tid & 1) * 16, v4u{0, 0, 0, 0});

  unsigned rt1 = 0, ks1 = 0;
  if (isG1) {
    const unsigned mt = (xcd >> 2) * 4 + (r & 3), nt = (xcd & 3) * 7 + (r >> 2);
    const unsigned koff = 2 * (xcd & 3) + 4 * (xcd >> 2);
    const unsigned mq = w >> 1, nh = w & 1;
    rt1 = mt * 4 + mq; ks1 = nt * 2 + nh;
    const unsigned vox = (8 * w + (l >> 3)) * 1024 + (((l & 7) ^ ((4 * (w & 1) + (l >> 4)) & 7)) << 4);
    const unsigned vow = (8 * w + (l >> 3)) * 3584 + ((((l >> 1) & 3) ^ (((l >> 4) & 1) | ((w & 1) << 1))) << 5) + ((l & 1) << 4);
    const unsigned sx0 = mt * 262144, sw0 = nt * 128, lw = w * 1024;
#define KST(s) (((s) + koff) & 7)
#define LD_X(s, slot, i) glds(vox, rq, sx0 + (i) * 65536 + KST(s) * 128, (slot) + lw + (i) * 8192)
#define LD_G(s, slot) glds(vow, rwg, sw0 + KST(s) * 229376, (slot) + lw + 32768)
#define LD_U(s, slot) glds(vow, rwu, sw0 + KST(s) * 229376, (slot) + lw + 40960)
    LD_X(0, 0, 0); LD_G(0, 0); LD_U(0, 0); LD_X(0, 0, 1); LD_X(0, 0, 2); LD_X(0, 0, 3);
    LD_X(1, SLOT, 0); LD_G(1, SLOT); LD_U(1, SLOT); LD_X(1, SLOT, 1); LD_X(1, SLOT, 2); LD_X(1, SLOT, 3);
    LD_X(2, 2 * SLOT, 0); LD_X(2, 2 * SLOT, 1); LD_G(2, 2 * SLOT);
    const unsigned ax0 = (64 * mq + (l & 15)) * 128 + ((g ^ ((l & 15) >> 1)) << 4), ax1 = ax0 ^ 64;
    const unsigned aw0 = 32768 + (8 * g + qq) * 128 + (((2 * nh) ^ ((qq >> 1) | ((g & 1) << 1))) << 5) + pp * 8, aw1 = aw0 ^ 32;
    v4f acc[4][4];
#pragma unroll
    for (int i = 0; i < 4; ++i)
#pragma unroll
      for (int j = 0; j < 4; ++j) acc[i][j] = v4f{0, 0, 0, 0};
    v4u X0[4], W0[4], X1[4], W1[4];
#define RD_X(dst, i, base) dst[i] = lds_r128((base) + (i) * 2048)
#define RD_W(dst, t, b0, b1, ks) dst[t] = lds_tr2(((t) & 1 ? (b1) : (b0)) + ((t) >> 1) * 8192 + (ks) * 4096, ((t) & 1 ? (b1) : (b0)) + ((t) >> 1) * 8192 + (ks) * 4096 + 512)
#define MM(X, W, i, j) acc[i][j] = mfma(W[j], X[i], acc[i][j])
#define HALF(X, W, XN, WN, bx, b0, b1, ks, RD, L0, L1, L2) do { \
      MM(X, W, 0, 0); if (RD) RD_W(WN, 0, b0, b1, ks); SB(); \
      MM(X, W, 0, 1); if (RD) RD_X(XN, 0, bx); SB(); \
      MM(X, W, 0, 2); if (RD) RD_W(WN, 1, b0, b1, ks); SB(); \
      MM(X, W, 0, 3); L0; SB(); \
      MM(X, W, 1, 0); if (RD) RD_W(WN, 2, b0, b1, ks); SB(); \
      MM(X, W, 1, 1); if (RD) RD_X(XN, 1, bx); SB(); \
      MM(X, W, 1, 2); if (RD) RD_W(WN, 3, b0, b1, ks); SB(); \
      MM(X, W, 1, 3); L1; SB(); \
      MM(X, W, 2, 0); if (RD) RD_X(XN, 2, bx); SB(); \
      MM(X, W, 2, 1); if (RD) RD_X(XN, 3, bx); SB(); \
      MM(X, W, 2, 2); SB(); \
      MM(X, W, 2, 3); L2; SB(); \
      MM(X, W, 3, 0); MM(X, W, 3, 1); MM(X, W, 3, 2); MM(X, W, 3, 3); SB(); } while (0)
    WAITV(9); BAR();
    STAMP(1);
#pragma unroll
    for (int i = 0; i < 4; ++i) { RD_W(W0, i, aw0, aw1, 0); RD_X(X0, i, ax0); }
    SB();
    unsigned cur = 0, nxt = SLOT, prv = 2 * SLOT;
    for (unsigned s = 0; s < 5; ++s) {
      HALF(X0, W0, X1, W1, ax1 + cur, aw0 + cur, aw1 + cur, 1, 1, LD_X(s + 2, prv, 2), LD_X(s + 2, prv, 3), LD_U(s + 2, prv));
      WAITV(6); WAITL0(); BAR();
      HALF(X1, W1, X0, W0, ax0 + nxt, aw0 + nxt, aw1 + nxt, 0, 1, LD_X(s + 3, cur, 0), LD_X(s + 3, cur, 1), LD_G(s + 3, cur));
      unsigned t = cur; cur = nxt; nxt = prv; prv = t;
    }
    STAMP(2);
#define LD_D(pc, lds) glds(vod, rwd, sod + (pc) * 8192, lds)
    HALF(X0, W0, X1, W1, ax1 + 2 * SLOT, aw0 + 2 * SLOT, aw1 + 2 * SLOT, 1, 1, LD_X(7, SLOT, 2), LD_X(7, SLOT, 3), LD_U(7, SLOT));
    WAITV(6); WAITL0(); BAR();
    glds(l * 16, rssm, ssm_s, ssm_l);
    HALF(X1, W1, X0, W0, ax0, aw0, aw1, 0, 1, LD_D(0, ldA), LD_D(1, ldA + 1024), LD_D(2, ldA + 2048));
    HALF(X0, W0, X1, W1, ax1, aw0, aw1, 1, 1, LD_D(3, ldA + 3072), LD_D(4, ldA + 4096), LD_D(5, ldA + 5120));
    WAITV(7); WAITL0(); BAR();
    const unsigned pofs = l < 32 ? WS_FA + l * 4 : WS_EPOCH;
    unsigned p1;
    asm volatile("buffer_load_dword %0, %1, %2, 0 offen sc1" : "=v"(p1) : "v"(pofs), "s"(rws) : "memory");
    HALF(X1, W1, X0, W0, ax0 + SLOT, aw0 + SLOT, aw1 + SLOT, 0, 1, LD_D(6, ldB), LD_D(7, ldB + 1024), LD_D(8, ldB + 2048));
    HALF(X0, W0, X1, W1, ax1 + SLOT, aw0 + SLOT, aw1 + SLOT, 1, 1, LD_D(9, ldB + 3072), LD_D(10, ldB + 4096), LD_D(11, ldB + 5120));
    asm volatile("s_waitcnt vmcnt(6)" : "+v"(p1)::"memory");
    WAITL0(); BAR();
    tag = __builtin_amdgcn_readlane(p1, 32) + 1;
    if (tid == 0) st32(rws, WS_FE + b * 4, tag);
    for (unsigned sp = 0; !__all(l >= 32 || p1 == tag) && sp < SPIN_CAP; ++sp) { relax(); p1 = ld32(rws, pofs); }
    const unsigned om = WS_MEAN + (rt1 * 64 + l) * 8, oc = WS_CS + nt * 1024 + (l >> 5) * 512 + (nh * 32 + (l & 31)) * 8;
    v2u gm, gc;
    asm volatile("buffer_load_dwordx2 %0, %1, %2, 0 offen sc1" : "=v"(gm) : "v"(om), "s"(rws) : "memory");
    asm volatile("buffer_load_dwordx2 %0, %1, %2, 0 offen sc1" : "=v"(gc) : "v"(oc), "s"(rws) : "memory");
    const unsigned ou = WS_U + b * 8192 + tid * 16;
    asm volatile("buffer_load_dwordx4 %0, %1, %2, 0 offen sc1" : "=v"(uv) : "v"(ou), "s"(rws) : "memory");
    LD_D(12, ldC);
    HALF(X1, W1, X0, W0, 0, 0, 0, 0, 0, LD_D(13, ldC + 1024), LD_D(14, ldC + 2048), LD_D(15, ldC + 3072));
    STAMP(3);
    asm volatile("s_waitcnt vmcnt(4)" : "+v"(gm), "+v"(gc), "+v"(uv)::"memory");
    for (unsigned sp = 0; !__all(gm.y == tag && gc.y == tag) && sp < SPIN_CAP; ++sp) { relax(); gm = ld64(rws, om); gc = ld64(rws, oc); }
    STAMP(4);
    const unsigned scr = L_SCR + w * 512;
    lds_w32(scr + l * 4, gm.x);
    lds_w32(scr + 256 + l * 4, gc.x);
    v4f cs[4];
#pragma unroll
    for (int j = 0; j < 4; ++j) cs[j] = __builtin_bit_cast(v4f, lds_r128(scr + 256 + j * 64 + g * 16));
#pragma unroll
    for (int i = 0; i < 4; ++i) {
      const float m = -__uint_as_float(lds_r32(scr + i * 64 + (l & 15) * 4));
      float h[8];
#pragma unroll
      for (int e = 0; e < 8; ++e) {
        const float gt = fmaf(m, cs[e >> 2][e & 3], acc[i][e >> 2][e & 3]);
        const float up = fmaf(m, cs[2 + (e >> 2)][e & 3], acc[i][2 + (e >> 2)][e & 3]);
        h[e] = fmaxf(gt, 0.f) * up;
      }
      st128(rws, l * 16, WS_H + ((rt1 * 56 + ks1) * 4 + i) * 1024, v4u{pk(h[0], h[1]), pk(h[2], h[3]), pk(h[4], h[5]), pk(h[6], h[7])});
    }
    SB();
  } else {
    const unsigned al = r - 28, a = xcd * 4 + al;
    const rsrc_t rk = mk(p.k, 2u << 20), rv = mk(p.v, 2u << 20), rsk = mk(p.sk, 4096);
    {
      unsigned long long pc;
      asm volatile("s_getpc_b64 %0" : "=s"(pc));
      (void)pc;
    }
    tag = __builtin_amdgcn_readfirstlane(ld32(rws, WS_EPOCH)) + 1;
    if (tid == 0) st32(rws, WS_FE + b * 4, tag);
    v4u kk[11], vv[8], sk[4];
    const int t0 = a * 64 + w * 8;
#pragma unroll
    for (int i = 0; i < 4; ++i) sk[i] = ld128p(rsk, l * 64 + i * 16, 0);
#pragma unroll
    for (int i = 0; i < 11; ++i) {
      const int t = t0 - 3 + i;
      kk[i] = ld128p(rk, l * 16 + (t < 0 ? 0x7ff00000u : (unsigned)t * 1024), 0);
    }
#pragma unroll
    for (int i = 0; i < 8; ++i) vv[i] = ld128p(rv, l * 16, (t0 + i) * 1024);
    SB();
    const unsigned cidx = (xcd >> 2) * 4 + al;
    const unsigned cnt = (xcd & 3) * 7 + (cidx < 7 ? cidx : 0);
    const rsrc_t rwc = (w >> 2) ? rwu : rwg;
    const unsigned cvo = (l >> 3) * 3584 + (l & 7) * 16, cso = (w & 3) * 128 * 3584 + cnt * 128;
    v4u cw[16];
#pragma unroll
    for (int j = 0; j < 4; ++j) {
#pragma unroll
      for (int c = 0; c < 2; ++c) {
        const unsigned s01 = sk[j][2 * c], s23 = sk[j][2 * c + 1];
        const float s0 = lo(s01), s1 = hi(s01), s2 = lo(s23), s3 = hi(s23);
        float kf[11], o[8];
#pragma unroll
        for (int i = 0; i < 11; ++i) kf[i] = c ? hi(kk[i][j]) : lo(kk[i][j]);
#pragma unroll
        for (int i = 0; i < 8; ++i) {
          const float ks = fmaf(kf[i + 3], s0, fmaf(kf[i + 2], s1, fmaf(kf[i + 1], s2, kf[i] * s3)));
          o[i] = ks * (c ? hi(vv[i][j]) : lo(vv[i][j]));
        }
        lds_w128((8 * l + 2 * j + c) * 128 + ((w ^ (l & 7)) << 4), v4u{pk(o[0], o[1]), pk(o[2], o[3]), pk(o[4], o[5]), pk(o[6], o[7])});
      }
      SB();
#pragma unroll
      for (int i = 0; i < 4; ++i) cw[4 * j + i] = ld128p(rwc, cvo, cso + (4 * j + i) * 8 * 3584);
      SB();
    }
    STAMP(1);
    WAITL0(); BAR();
#pragma unroll
    for (int i = 0; i < 8; ++i) {
      const unsigned ch = 64 * w + 8 * i + (l >> 3);
      st128(rws, ch * 4096 + (l & 7) * 16, WS_U + a * 128, lds_r128(ch * 128 + (((l & 7) ^ i) << 4)));
    }
    SB();
    v4u xr[8];
#pragma unroll
    for (int i = 0; i < 8; ++i) xr[i] = ld128p(rq, l * 16, (t0 + i) * 1024);
    SB();
    float cs[8];
#pragma unroll
    for (int e = 0; e < 8; ++e) cs[e] = 0.f;
#pragma unroll
    for (int i = 0; i < 16; ++i)
#pragma unroll
      for (int j = 0; j < 4; ++j) { cs[2 * j] += lo(cw[i][j]); cs[2 * j + 1] += hi(cw[i][j]); }
#pragma unroll
    for (int e = 0; e < 8; ++e) { cs[e] += __shfl_xor(cs[e], 8); cs[e] += __shfl_xor(cs[e], 16); cs[e] += __shfl_xor(cs[e], 32); }
    if (l < 8) {
      lds_w128(65536 + w * 256 + l * 32, __builtin_bit_cast(v4u, v4f{cs[0], cs[1], cs[2], cs[3]}));
      lds_w128(65536 + w * 256 + l * 32 + 16, __builtin_bit_cast(v4u, v4f{cs[4], cs[5], cs[6], cs[7]}));
    }
    WAITV(8); WAITL0(); BAR();
    STAMP(2);
    if (tid == 0) st32(rws, WS_FA + a * 4, tag);
    if (tid < 128 && cidx < 7) {
      const unsigned ab = 65536 + (tid >> 6) * 1024 + l * 4;
      const float s = (__uint_as_float(lds_r32(ab)) + __uint_as_float(lds_r32(ab + 256))) + (__uint_as_float(lds_r32(ab + 512)) + __uint_as_float(lds_r32(ab + 768)));
      st64(rws, WS_CS + cnt * 1024 + tid * 8, v2u{__float_as_uint(s), tag});
    }
    float mean[8], mysel = 0.f;
#pragma unroll
    for (int i = 0; i < 8; ++i) {
      float s = 0.f;
#pragma unroll
      for (int j = 0; j < 4; ++j) s += lo(xr[i][j]) + hi(xr[i][j]);
      mean[i] = wsum(s) * (1.f / 512.f);
      mysel = l == (unsigned)i ? mean[i] : mysel;
    }
    if (l < 8) st64(rws, WS_MEAN + (t0 + l) * 8, v2u{__float_as_uint(mysel), tag});
    STAMP(3);
    WAITL0(); BAR();
    glds(l * 16, rssm, ssm_s, ssm_l);
#pragma unroll
    for (int i = 0; i < 6; ++i) glds(vod, rwd, sod + i * 8192, ldA + i * 1024);
#pragma unroll
    for (int i = 0; i < 8; ++i) {
      float s = 0.f;
#pragma unroll
      for (int j = 0; j < 4; ++j) { const float d0 = lo(xr[i][j]) - mean[i], d1 = hi(xr[i][j]) - mean[i]; s = fmaf(d0, d0, fmaf(d1, d1, s)); }
      const float rs = 1.f / sqrtf(wsum(s) * (1.f / 512.f) + 1e-5f);
      mysel = l == (unsigned)i ? rs : mysel;
    }
    if (l < 8) st64(rws, WS_RSTD + (t0 + l) * 8, v2u{__float_as_uint(mysel), tag});
#pragma unroll
    for (int i = 0; i < 6; ++i) glds(vod, rwd, sod + (6 + i) * 8192, ldB + i * 1024);
#pragma unroll
    for (int i = 0; i < 4; ++i) glds(vod, rwd, sod + (12 + i) * 8192, ldC + i * 1024);
    unsigned p1 = ld32(rws, WS_FA + (l & 31) * 4);
    for (unsigned sp = 0; !__all(p1 == tag) && sp < SPIN_CAP; ++sp) { relax(); p1 = ld32(rws, WS_FA + (l & 31) * 4); }
    uv = ld128(rws, tid * 16, WS_U + b * 8192);
    asm volatile("s_waitcnt vmcnt(0)" : "+v"(uv)::"memory");
    BAR();
    STAMP(4);
  }
  STAMP(5);
  lds_w128(L_U + (tid >> 8) * 4576 + 480 + (tid & 255) * 16, uv);
  if (tid < 62) lds_w128(tid < 60 ? L_U + (tid >= 30 ? 4576 - 480 : 0) + tid * 16 : L_CARRY + (tid - 60) * 528, v4u{0, 0, 0, 0});
  v4u WD[7][4];
  const unsigned adr = (4 * g + qq) * 128 + pp * 8, afx = ((2 * g + (qq >> 1)) & 3) << 5;
#define PIECE(pc) ((pc) < 6 ? ldA + (pc) * 1024 : (pc) < 12 ? ldB + ((pc) - 6) * 1024 : (pc) < 16 ? ldC + ((pc) - 12) * 1024 : (pc) < 22 ? ldA + ((pc) - 16) * 1024 : ldB + ((pc) - 22) * 1024)
#define RD_D(ks) _Pragma("unroll") for (int n = 0; n < 4; ++n) WD[ks][n] = lds_tr2(PIECE(4 * (ks)) + adr + ((n << 5) ^ afx), PIECE(4 * (ks) + 2) + adr + ((n << 5) ^ afx))
  RD_D(0); RD_D(1); RD_D(2);
  const unsigned ch = w >> 2, c = w & 3;
  v4u H[16];
  const unsigned aH = L_SP + ch * 4128 + 224 + 8 * pp - 64 * g - 8 * qq;
#pragma unroll
  for (int z = 0; z < 16; ++z) H[z] = lds_tr2(aH + 256 * z + 32, aH + 256 * z);
  SB(); WAITL0(); SB();
#pragma unroll
  for (int i = 16; i < 28; ++i) glds(vod, rwd, sod + i * 8192, PIECE(i));
  BAR();
  STAMP(6);
  v4f ya[8];
#pragma unroll
  for (int i = 0; i < 8; ++i) ya[i] = v4f{0, 0, 0, 0};
  const unsigned aU = L_U + ch * 4576 + 128 * c + 32 * pp + 64 * g + 8 * qq;
  const unsigned aY = L_SP + ch * 8192 + c * 256 + l * 4;
  const unsigned oh = WS_FH + rt * 256 + w * 32 + (l < 7 ? l : 0) * 4;
  unsigned p2;
  {
    v4u Ua = lds_tr2(aU, aU + 32), Ub = lds_tr2(aU + 256, aU + 256 + 32);
#pragma unroll
    for (int j = 0; j < 16; ++j) {
      const v4u Uc = Ua;
      Ua = Ub;
      if (j + 2 < 16) Ub = lds_tr2(aU + 256 * (j + 2), aU + 256 * (j + 2) + 32);
#pragma unroll
      for (int v = j >> 1; v < 8; ++v) ya[v] = mfma(Uc, H[2 * v + 1 - j], ya[v]);
      if (j >= 2 && !(j & 1)) {
        const int v = (j >> 1) - 1;
        ya[v][2] += shr1(ya[v][3]); ya[v][1] += shr1(ya[v][2]); ya[v][0] += shr1(ya[v][1]);
        lds_w32(aY + v * 1024, __float_as_uint(ya[v][0]));
      }
      SB();
      if (j == 3) {
        WAITV(12);
        if (isG1 && l == 0) st32(rws, WS_FH + rt1 * 256 + (ks1 / 7) * 32 + (ks1 % 7) * 4, tag);
        SB();
      }
      if (j == 11) { asm volatile("buffer_load_dword %0, %1, %2, 0 offen sc1" : "=v"(p2) : "v"(oh), "s"(rws) : "memory"); SB(); }
    }
    ya[7][2] += shr1(ya[7][3]); ya[7][1] += shr1(ya[7][2]); ya[7][0] += shr1(ya[7][1]);
    lds_w32(aY + 7 * 1024, __float_as_uint(ya[7][0]));
    if (l == 63) {
#pragma unroll
      for (int v = 0; v < 8; ++v) lds_w128(L_CARRY + ch * 528 + (4 * v + c + 1) * 16, __builtin_bit_cast(v4u, v4f{ya[v][1], ya[v][2], ya[v][3], 0.f}));
    }
  }
  STAMP(7);
  asm volatile("s_waitcnt vmcnt(0)" : "+v"(p2)::"memory");
  for (unsigned sp = 0; !__all(p2 == tag) && sp < SPIN_CAP; ++sp) { relax(); p2 = ld32(rws, oh); }
  STAMP(8);
  const unsigned rot = (ct & 1) * 2;
  const unsigned sh = WS_H + (rt * 56 + w * 7) * 4096;
  v4u Ha[7], Hb[7];
#define LD_H(dst, mi) _Pragma("unroll") for (int ks = 0; ks < 7; ++ks) dst[ks] = ld128(rws, l * 16 + ks * 4096, sh + (((mi) ^ rot) << 10))
  LD_H(Ha, 0);
  SB();
  RD_D(3); RD_D(4); RD_D(5); RD_D(6);
  SB(); WAITL0(); BAR();
  {
    const unsigned sdp = *(const unsigned*)(p.sd + 2 * b);
    const float d0 = lo(sdp), d1 = hi(sdp);
#pragma unroll
    for (int i = 0; i < 2; ++i) {
      const unsigned pi = tid + 512 * i;
      const unsigned ca = L_CARRY + ((pi & 31) < 2 ? (pi >> 5) * 16 + (pi & 31) * 8 : 0);
      const v2f y0 = __builtin_bit_cast(v2f, lds_r64(L_SP + pi * 8)) + __builtin_bit_cast(v2f, lds_r64(ca));
      const v2f y1 = __builtin_bit_cast(v2f, lds_r64(L_SP + 8192 + pi * 8)) + __builtin_bit_cast(v2f, lds_r64(ca + 528));
      const unsigned u0 = lds_r32(L_U + 480 + pi * 4), u1 = lds_r32(L_U + 4576 + 480 + pi * 4);
      st128(rws, pi * 16, WS_Z + b * 16384, v4u{pk(fmaf(lo(u0), d0, y0.x), fmaf(lo(u1), d1, y1.x)), tag, pk(fmaf(hi(u0), d0, y0.y), fmaf(hi(u1), d1, y1.y)), tag});
    }
  }
  SB();
  STAMP(9);
  LD_H(Hb, 1);
  SB();
#define REDB(wv, mi, n) ((mi) == 0 ? 2 * SLOT + (wv) * 6144 + (n) * 1024 : (mi) == 1 ? (wv) * 6144 + (n) * 1024 : (mi) == 2 ? SLOT + (wv) * 4096 + (n) * 1024 \
                         : ((n) < 2 ? 2 * SLOT : 0) + (wv) * 6144 + 4096 + ((n) & 1) * 1024)
#define G2(Hx, mi) do { v4f o[4]; \
    _Pragma("unroll") for (int n = 0; n < 4; ++n) o[n] = v4f{0, 0, 0, 0}; \
    _Pragma("unroll") for (int ks = 0; ks < 7; ++ks) _Pragma("unroll") for (int n = 0; n < 4; ++n) o[n] = mfma(WD[ks][n], Hx[ks], o[n]); \
    _Pragma("unroll") for (int n = 0; n < 4; ++n) lds_w128(REDB(w, mi, n) + ((l ^ (((2 * n + (g >> 1)) & 3) << 2)) << 4), __builtin_bit_cast(v4u, o[n])); \
    SB(); } while (0)
  G2(Ha, 0);
  STAMP(10);
  LD_H(Ha, 2); SB();
  G2(Hb, 1);
  LD_H(Hb, 3); SB();
  const unsigned mloc = w >> 1;
  const unsigned row = rt * 64 + ((mloc ^ rot) << 4) + (w & 1) * 8 + (l >> 3), cg = l & 7;
  const v4u xv = ld128p(rq, row * 1024 + cg * 16, ct * 128);
  const unsigned oz = WS_Z + (ct * 32 + w * 4 + (l >> 5)) * 16384 + rt * 512 + (l & 31) * 16;
  v4u z0 = ld128(rws, oz, 0), z1 = ld128(rws, oz, 32768);
  v2u gm = ld64(rws, WS_MEAN + row * 8), gr = ld64(rws, WS_RSTD + row * 8);
  v4u fe = v4u{tag, tag, tag, tag};
  if (b == 0 && w == 7) fe = ld128(rws, WS_FE + l * 16, 0);
  SB();
  G2(Ha, 2);
  G2(Hb, 3);
  STAMP(11);
  for (unsigned sp = 0; !__all(z0.y == tag && z0.w == tag && z1.y == tag && z1.w == tag && gm.y == tag && gr.y == tag) && sp < SPIN_CAP; ++sp) {
    relax();
    z0 = ld128(rws, oz, 0); z1 = ld128(rws, oz, 32768); gm = ld64(rws, WS_MEAN + row * 8); gr = ld64(rws, WS_RSTD + row * 8);
  }
  {
    BAR();
    const unsigned za = L_U + ((l & 31) * 2 * 33 + w * 4 + (l >> 5)) * 4;
    lds_w32(za, z0.x); lds_w32(za + 132, z0.z); lds_w32(za + 8, z1.x); lds_w32(za + 140, z1.z);
  }
  WAITL0(); BAR();
  {
    const unsigned tok = (w & 1) * 8 + (l >> 3), n = cg >> 1, g0 = (cg & 1) * 2;
    const unsigned o0 = ((g0 * 16 + tok) ^ ((cg & 3) << 2)) << 4;
    const unsigned base = (mloc == 0 ? 2 * SLOT + n * 1024 : mloc == 1 ? n * 1024 : mloc == 2 ? SLOT + n * 1024 : (n < 2 ? 2 * SLOT : 0) + 4096 + (n & 1) * 1024) + o0;
    const unsigned str = mloc == 2 ? 4096 : 6144;
    v4f s0 = v4f{0, 0, 0, 0}, s1 = v4f{0, 0, 0, 0};
#pragma unroll
    for (int i = 0; i < 8; ++i) {
      s0 += __builtin_bit_cast(v4f, lds_r128(base + i * str));
      s1 += __builtin_bit_cast(v4f, lds_r128(base + i * str + 256));
    }
    const unsigned zr = L_U + ((row & 63) * 33 + cg * 4) * 4;
    const float mean = __uint_as_float(gm.x), rstd = __uint_as_float(gr.x), r2 = rstd * rstd;
    v4u o;
#pragma unroll
    for (int j = 0; j < 4; ++j) {
      const unsigned zz = lds_r32(zr + j * 4);
      const float x0 = lo(xv[j]), x1 = hi(xv[j]);
      const float b0 = j < 2 ? s0[2 * j] : s1[2 * j - 4], b1 = j < 2 ? s0[2 * j + 1] : s1[2 * j - 3];
      o[j] = pk(fmaf(r2, b0, fmaf((x0 - mean) * rstd, lo(zz), x0)), fmaf(r2, b1, fmaf((x1 - mean) * rstd, hi(zz), x1)));
    }
    *(v4u*)((char*)p.out + row * 1024 + ct * 128 + cg * 16) = o;
  }
  if (b == 0 && w == 7) {
    for (unsigned sp = 0; !__all(fe.x == tag && fe.y == tag && fe.z == tag && fe.w == tag) && sp < SPIN_CAP; ++sp) { relax(); fe = ld128(rws, WS_FE + l * 16, 0); }
    if (l == 0) st32(rws, WS_EPOCH, tag);
  }
  STAMP(12);
  if (true) { unsigned d_ = T[12] - T[0]; if (d_ > 10000) d_ = 10000; while ((unsigned)__builtin_amdgcn_s_memrealtime() - T[0] < 0) __builtin_amdgcn_s_sleep(4); }
}

extern "C" void kernel_launch(void* const* d_in, const int* in_sizes, int n_in, void* d_out, int out_size, void* d_ws, size_t ws_size,
                              hipStream_t stream) {
  Params p;
  p.q = (const u16*)d_in[0]; p.k = (const u16*)d_in[1]; p.v = (const u16*)d_in[2]; p.sk = (const u16*)d_in[3];
  p.ssm = (const u16*)d_in[4]; p.sd = (const u16*)d_in[5]; p.wg = (const u16*)d_in[7]; p.wu = (const u16*)d_in[8];
  p.wd = (const u16*)d_in[9]; p.out = (u16*)d_out; p.ws = (char*)d_ws;
  (void)hipFuncSetAttribute((const void*)fused_kernel, hipFuncAttributeMaxDynamicSharedMemorySize, LDS_BYTES);
  fused_kernel<<<256, 512, LDS_BYTES, stream>>>(p);
}
```

```cpp
#include <hip/hip_runtime.h>
#include <stdint.h>

typedef unsigned short u16;
typedef unsigned v2u __attribute__((ext_vector_type(2)));
typedef unsigned v4u __attribute__((ext_vector_type(4)));
typedef float v2f __attribute__((ext_vector_type(2)));
typedef float v4f __attribute__((ext_vector_type(4)));
typedef short v4s __attribute__((ext_vector_type(4)));
typedef __bf16 v2bf __attribute__((ext_vector_type(2)));
typedef __bf16 v8bf __attribute__((ext_vector_type(8)));
typedef __amdgpu_buffer_rsrc_t rsrc_t;
#define LAS __attribute__((address_space(3)))
#define DEV __device__ __forceinline__

constexpr unsigned WS_EPOCH = 0;
constexpr unsigned WS_FA = 4096;
constexpr unsigned WS_STAT = 40960;
constexpr unsigned WS_FH = 81920;
constexpr unsigned WS_CS = 262144;
constexpr unsigned WS_U = 2u << 20;
constexpr unsigned WS_Z = 4u << 20;
constexpr unsigned WS_P = 8u << 20;
constexpr unsigned WS_H = 12u << 20;
constexpr unsigned WS_END = WS_H + 2048u * 1792u * 2u;
constexpr unsigned SLOT = 49152, L_SP = 147456, LDS_BYTES = 163840;
constexpr unsigned L_U = 2 * SLOT + 32768;
constexpr unsigned L_CARRY = L_U + 9216;
constexpr unsigned L_Y = SLOT + 32768;
constexpr unsigned L_STAT = L_SP + 8448;
constexpr unsigned L_DUMMY = L_SP + 15872;
constexpr int SC1 = 16;
#define NW5 3
#define X0_AT 13
#define D_AT 6
#define KX2 5
constexpr unsigned SPIN_CAP = 1u << 16;
constexpr unsigned CODE_BYTES = 26624;

struct Params {
  const u16 *q, *k, *v, *sk, *ssm, *sd, *wg, *wu, *wd;
  u16* out;
  char* ws;
};

DEV rsrc_t mk(const void* p, unsigned bytes) { return __builtin_amdgcn_make_buffer_rsrc((void*)p, (short)0, (int)bytes, 0x00020000); }
DEV void glds(unsigned voff, rsrc_t rs, unsigned soff, unsigned lds) {
  asm volatile("s_mov_b32 m0, %3\n\ts_nop 0\n\tbuffer_load_dwordx4 %0, %1, %2 offen lds" ::"v"(voff), "s"(rs), "s"(soff), "s"(lds) : "memory");
}
#define WAITV(n) asm volatile("s_waitcnt vmcnt(" #n ")" ::: "memory")
#define WAITVX(n) asm volatile("s_waitcnt vmcnt(%0)" ::"i"(n) : "memory")
#define WAITV0() do { WAITV(0); __builtin_amdgcn_s_waitcnt(0x0f70); } while (0)
#define WAITL0() asm volatile("s_waitcnt lgkmcnt(0)" ::: "memory")
#define BAR() do { __builtin_amdgcn_sched_barrier(0); __builtin_amdgcn_s_barrier(); __builtin_amdgcn_sched_barrier(0); } while (0)
#define SB() __builtin_amdgcn_sched_barrier(0)
DEV v4u lds_r128(unsigned a) { return *(const LAS v4u*)(a); }
DEV v2u lds_r64(unsigned a) { return *(const LAS v2u*)(a); }
DEV unsigned lds_r32(unsigned a) { return *(const LAS unsigned*)(a); }
DEV void lds_w128(unsigned a, v4u v) { *(LAS v4u*)(a) = v; }
DEV void lds_w32(unsigned a, unsigned v) { *(LAS unsigned*)(a) = v; }
DEV v2u lds_tr(unsigned a) { return __builtin_bit_cast(v2u, __builtin_amdgcn_ds_read_tr16_b64_v4i16((LAS v4s*)(a))); }
DEV v4u lds_tr2(unsigned a0, unsigned a1) { v2u x = lds_tr(a0), y = lds_tr(a1); return v4u{x.x, x.y, y.x, y.y}; }
DEV v4f mfma(v4u a, v4u b, v4f c) { return __builtin_amdgcn_mfma_f32_16x16x32_bf16(__builtin_bit_cast(v8bf, a), __builtin_bit_cast(v8bf, b), c, 0, 0, 0); }
#define ACC_LIST(X) X(0, 128, 131) X(1, 132, 135) X(2, 136, 139) X(3, 140, 143) X(4, 144, 147) X(5, 148, 151) X(6, 152, 155) X(7, 156, 159) X(8, 160, 163) X(9, 164, 167) X(10, 168, 171) X(11, 172, 175) X(12, 176, 179) X(13, 180, 183) X(14, 184, 187) X(15, 188, 191) X(16, 192, 195) X(17, 196, 199) X(18, 200, 203) X(19, 204, 207) X(20, 208, 211) X(21, 212, 215) X(22, 216, 219) X(23, 220, 223) X(24, 224, 227) X(25, 228, 231) X(26, 232, 235) X(27, 236, 239) X(28, 240, 243) X(29, 244, 247) X(30, 248, 251) X(31, 252, 255)
template <int I> DEV void acc_mfma(v4f& c, const v4u& a, const v4u& b) {
#define X(i, lo_, hi_) if constexpr (I == i) asm volatile("v_mfma_f32_16x16x32_bf16 %0, %1, %2, %0" : "+{v[" #lo_ ":" #hi_ "]}"(c) : "v"(a), "v"(b));
  ACC_LIST(X)
#undef X
}
template <int I, int E> DEV void acc_fma(v4f& c, float m, float c0) {
#define X(i, lo_, hi_) if constexpr (I == i) asm volatile("v_fmac_f32 v[%c3], %1, %2" : "+{v[" #lo_ ":" #hi_ "]}"(c) : "v"(m), "v"(c0), "i"(lo_ + E));
  ACC_LIST(X)
#undef X
}
DEV void mfma_acc(v4f& c, const v4u& a, const v4u& b) { asm volatile("v_mfma_f32_16x16x32_bf16 %0, %1, %2, %0" : "+v"(c) : "v"(a), "v"(b)); }
DEV void mfma_new(v4f& c, const v4u& a, const v4u& b) { asm volatile("v_mfma_f32_16x16x32_bf16 %0, %1, %2, 0" : "=&v"(c) : "v"(a), "v"(b)); }
template <int I> DEV void acc_pin(v4f& c) {
#define X(i, lo_, hi_) if constexpr (I == i) asm volatile("" : "+{v[" #lo_ ":" #hi_ "]}"(c));
  ACC_LIST(X)
#undef X
}
template <int N> struct IC { static constexpr int value = N; };
template <int N, int I = 0, class F> DEV void sfor(F&& f) { if constexpr (I < N) { f(IC<I>{}); sfor<N, I + 1>(f); } }
DEV float lo(unsigned d) { return __uint_as_float(d << 16); }
DEV float hi(unsigned d) { return __uint_as_float(d & 0xffff0000u); }
DEV unsigned pk(float a, float b) { v2bf r = {(__bf16)a, (__bf16)b}; return __builtin_bit_cast(unsigned, r); }
DEV float fma1(float a, float b, float c) { float d = fmaf(a, b, c); asm("" : "+v"(d)); return d; }
DEV float add1(float a, float b) { float d = a + b; asm("" : "+v"(d)); return d; }
DEV void glu2a(float& t0, float& t1, float g0, float g1) { asm("v_max_f32 %0, 0, %2\n\tv_max_f32 %1, 0, %3" : "=&v"(t0), "=v"(t1) : "v"(g0), "v"(g1)); }
DEV unsigned glu2b(float t0, float t1, float u0, float u1) {
  unsigned o;
  asm("v_mul_f32 %0, %2, %3\n\tv_mul_f32 %1, %1, %4\n\tv_cvt_pk_bf16_f32 %0, %0, %1" : "=&v"(o), "+v"(t1) : "v"(t0), "v"(u0), "v"(u1));
  return o;
}
DEV unsigned pin(unsigned x) { asm volatile("" : "+v"(x)); return x; }
DEV float shr1(float x) { return __int_as_float(__builtin_amdgcn_update_dpp(0, __float_as_int(x), 0x138, 0xf, 0xf, true)); }
template <int C> DEV float dpp(float x) { return __int_as_float(__builtin_amdgcn_update_dpp(0, __float_as_int(x), C, 0xf, 0xf, true)); }
DEV float xsum16(float x) { auto r = __builtin_amdgcn_permlane16_swap(__float_as_uint(x), __float_as_uint(x), false, false); return __uint_as_float(r[0]) + __uint_as_float(r[1]); }
DEV float xsum32(float x) { auto r = __builtin_amdgcn_permlane32_swap(__float_as_uint(x), __float_as_uint(x), false, false); return __uint_as_float(r[0]) + __uint_as_float(r[1]); }
DEV float wsum(float x) {
  x += dpp<0xb1>(x); x += dpp<0x4e>(x); x += dpp<0x141>(x); x += dpp<0x140>(x);
  return xsum32(xsum16(x));
}
#define ALD32(d, off, rs) asm volatile("buffer_load_dword %0, %1, %2, 0 offen sc1" : "=v"(d) : "v"(off), "s"(rs) : "memory")
#define ALD64(d, off, rs) asm volatile("buffer_load_dwordx2 %0, %1, %2, 0 offen sc1" : "=v"(d) : "v"(off), "s"(rs) : "memory")
#define ALD128(d, off, rs, so) asm volatile("buffer_load_dwordx4 %0, %1, %2, %3 offen sc1" : "=v"(d) : "v"(off), "s"(rs), "s"(so) : "memory")
#define ALD128P(d, off, rs, so) asm volatile("buffer_load_dwordx4 %0, %1, %2, %3 offen" : "=v"(d) : "v"(off), "s"(rs), "s"(so) : "memory")
#define RLD32(d, off, rs) asm volatile("s_sleep 1\n\tbuffer_load_dword %0, %1, %2, 0 offen sc1\n\ts_waitcnt vmcnt(0)" : "=&v"(d) : "v"(off), "s"(rs) : "memory")
#define RLD64(d, off, rs) asm volatile("s_sleep 1\n\tbuffer_load_dwordx2 %0, %1, %2, 0 offen sc1\n\ts_waitcnt vmcnt(0)" : "=&v"(d) : "v"(off), "s"(rs) : "memory")
#define RLD128(d, off, rs) asm volatile("s_sleep 1\n\tbuffer_load_dwordx4 %0, %1, %2, 0 offen sc1\n\ts_waitcnt vmcnt(0)" : "=&v"(d) : "v"(off), "s"(rs) : "memory")
#define RLD128X2(d0, o0, d1, o1, rs) asm volatile("s_sleep 1\n\tbuffer_load_dwordx4 %0, %2, %4, 0 offen sc1\n\tbuffer_load_dwordx4 %1, %3, %4, 0 offen sc1\n\ts_waitcnt vmcnt(0)" \
                                                   : "=&v"(d0), "=&v"(d1) : "v"(o0), "v"(o1), "s"(rs) : "memory")
#define SPIN(ok, reload) do { if (__builtin_expect(!__all(ok), 0)) { unsigned sp_ = 0; do { reload; } while (!__all(ok) && ++sp_ < SPIN_CAP); } } while (0)
DEV unsigned ld32(rsrc_t r, unsigned off) { return __builtin_amdgcn_raw_buffer_load_b32(r, off, 0, SC1); }
DEV v2u ld64(rsrc_t r, unsigned off) { return __builtin_amdgcn_raw_buffer_load_b64(r, off, 0, SC1); }
DEV v4u ld128(rsrc_t r, unsigned off, unsigned soff) { return __builtin_amdgcn_raw_buffer_load_b128(r, off, soff, SC1); }
DEV v4u ld128p(rsrc_t r, unsigned off, unsigned soff) { return __builtin_amdgcn_raw_buffer_load_b128(r, off, soff, 0); }
DEV void st32(rsrc_t r, unsigned off, unsigned v) { __builtin_amdgcn_raw_buffer_store_b32(v, r, off, 0, SC1); }
DEV void st64(rsrc_t r, unsigned off, v2u v) { __builtin_amdgcn_raw_buffer_store_b64(v, r, off, 0, SC1); }
DEV void st128(rsrc_t r, unsigned off, unsigned soff, v4u v) { __builtin_amdgcn_raw_buffer_store_b128(v, r, off + soff, 0, SC1); }

#define STAMP(i)

DEV void row_stats(const v4u& x, float& mean, float& var) {
  float f[8];
#pragma unroll
  for (int j = 0; j < 8; ++j) f[j] = (j & 1) ? hi(x[j >> 1]) : lo(x[j >> 1]);
  mean = wsum(add1(add1(add1(f[0], f[1]), add1(f[2], f[3])), add1(add1(f[4], f[5]), add1(f[6], f[7])))) * (1.f / 512.f);
  float s = 0.f;
#pragma unroll
  for (int j = 0; j < 8; ++j) { const float d = add1(f[j], -mean); s = fma1(d, d, s); }
  var = wsum(s) * (1.f / 512.f);
}

__global__ __launch_bounds__(512, 2) void fused_kernel(Params p) {
  const unsigned tid = threadIdx.x, l = tid & 63;
  const unsigned w = __builtin_amdgcn_readfirstlane(tid >> 6);
  const unsigned b = blockIdx.x, xcd = b & 7, r = b >> 3;
  const bool isG1 = r < 28;
  if (w == 0) {
    unsigned long long pc;
    asm volatile("s_getpc_b64 %0" : "=s"(pc));
    asm volatile("s_mov_b32 m0, %2\n\ts_nop 0\n\tglobal_load_lds_dword %0, %1" ::"v"(min((r * 8 + (l & 7)) * 128, CODE_BYTES)), "s"(pc), "s"(L_DUMMY) : "memory");
  }
  const rsrc_t rq = mk(p.q, 2u << 20), rws = mk(p.ws, WS_END), rwd = mk(p.wd, 1792u * 512u * 2u), rssm = mk(p.ssm, 2u << 20);
  const unsigned g = l >> 4, qq = (l >> 2) & 3, pp = l & 3;
  const unsigned kh = xcd & 1, rt = (xcd >> 1) * 8 + (r >> 3) * 2 + kh, ct = r & 7;
#define VOD ((l >> 3) * 1024 + ((((l >> 1) & 3) ^ ((l >> 4) & 3)) << 5) + ((l & 1) << 4))
#define ADR(n) ((4 * g + qq) * 128 + pp * 8 + ((((n) ^ (2 * g + (qq >> 1))) & 3) << 5))
#define PIECE(wv, pc) ((pc) < 8 ? 2 * SLOT + (wv) * 8192 + (pc) * 1024 : (pc) < 20 ? (wv) * 12288 + ((pc) - 8) * 1024 : SLOT + (wv) * 8192 + ((pc) - 20) * 1024)
#define LD_D(wv, a, e) _Pragma("unroll") for (int i_ = a; i_ < e; ++i_) glds(vod, rwd, (kh * 4 + (wv)) * 229376 + ct * 128 + i_ * 8192, PIECE(wv, i_))
#define LD_SSM(wv) glds(l * 16, rssm, b * 8192 + (wv) * 1024, L_SP + ((wv) >> 2) * 4128 + 32 + ((wv) & 3) * 1024)
#define NOP_ do {} while (0)
  unsigned p2;
  const unsigned oh = WS_FH + ((rt >> 1) * 56 + kh * 28 + (w & 3) * 7 + (l < 7 ? l : 0)) * 128;
  const unsigned lw = w & 3, lt = tid & 255;
  const unsigned pa = WS_FA + (l & 31) * 128;
  unsigned p1;
#define PF(rs, vo, so) asm volatile("s_mov_b32 m0, %3\n\ts_nop 0\n\tbuffer_load_dword %0, %1, %2 offen lds" ::"v"(vo), "s"(rs), "s"(so), "s"(L_DUMMY) : "memory")
#define LD_U() v4u uu[4]; do { _Pragma("unroll") for (int i = 0; i < 4; ++i) ALD128(uu[i], (l >> 3) * 65536 + (l & 7) * 16, rws, WS_U + b * 256 + (lw >> 1) * 128 + i * 524288); } while (0)
#define ST_U() do { SB(); asm volatile("" : "+v"(uu[0]), "+v"(uu[1]), "+v"(uu[2]), "+v"(uu[3])); \
    const unsigned lu = L_U + (lw >> 1) * 4320; \
    _Pragma("unroll") for (int i = 0; i < 4; ++i) lds_w128(lu + 224 + i * 1024 + l * 16, uu[i]); \
    lds_w32(l < 56 ? lu + l * 4 : L_CARRY + (lw >> 1) * 528 + (l & 3) * 4, 0); } while (0)
  unsigned tag;
  unsigned hflag = 0x7ff00000u;
  v4u WD[7][4];
  const unsigned srow = xcd * 256 + r * 8 + lw * 2;
  unsigned sdp = *(const __attribute__((address_space(4))) unsigned*)(unsigned long long)(p.sd + 2 * b);
  unsigned ep;
  v4u x0, x1;
  if (w < 4) { ALD32(ep, WS_EPOCH, rws); ALD128P(x0, l * 16, rq, srow * 1024); ALD128P(x1, l * 16, rq, srow * 1024 + 1024); }
#define ROWSTATS() do { float m0, v0, m1, v1; \
    SB(); asm volatile("" : "+v"(ep), "+v"(x0), "+v"(x1)); \
    row_stats(x0, m0, v0); row_stats(x1, m1, v1); \
    tag = __builtin_amdgcn_readfirstlane(ep) + 1; \
    const float rs = __builtin_amdgcn_rsqf(((l & 2) ? v1 : v0) + 1e-5f); \
    st64(rws, l < 4 ? WS_STAT + srow * 16 + l * 8 : 0x7ff00000u, v2u{__float_as_uint((l & 1) ? rs : (l & 2) ? m1 : m0), tag}); \
    lds_w32(l < 16 ? L_SP + (l >> 3) * 4128 + (l & 7) * 4 : L_STAT + 2048, l < 16 ? 0 : tag); } while (0)
  const unsigned rot = ct & 1;
  const unsigned sh = WS_H + (kh * 28 + lw * 7) * 4096 + (w >> 2) * 2048;
  v4u Hf[4][7];
#define LD_H1(st, ks) Hf[st][ks] = ld128(rws, l * 16, sh + ((st) < 2 ? rt ^ 1 : rt) * 229376 + (ks) * 4096 + ((((st) & 1) ^ rot) << 10))
#define LD_H(st) _Pragma("unroll") for (int ks = 0; ks < 7; ++ks) LD_H1(st, ks)

  if (__builtin_expect(isG1, 1)) {
    const unsigned mt = (xcd >> 2) * 4 + (r & 3), nt = (xcd & 3) * 7 + (r >> 2);
    if (__builtin_expect(w >= 4, 1)) {
      const rsrc_t rwg = mk(p.wg, 1792u * 512u * 2u), rwu = mk(p.wu, 1792u * 512u * 2u);
      const unsigned kx = (((xcd ^ (xcd >> 2)) & 1) << 2) | ((r + (r >> 2)) & 3);
      const unsigned vox = (8 * lw + (l >> 3)) * 1024 + (((l & 7) ^ ((4 * (lw & 1) + (l >> 4)) & 7)) << 4);
      const unsigned vow = (8 * lw + (l >> 3)) * 3584 + ((((l >> 1) & 3) ^ (((l >> 4) & 1) | ((lw & 1) << 1))) << 5) + ((l & 1) << 4);
#define RANGE(s, a, e) do { const unsigned ks_ = (s) ^ ((s) < 4 ? kx : kx & KX2), sx_ = mt * 262144 + ks_ * 128, sw_ = nt * 128 + ks_ * 229376, ld_ = ((s) % 3) * SLOT + lw * 1024; \
        _Pragma("unroll") for (int i_ = a; i_ < e; ++i_) { \
          if (i_ < 4) glds(vow, (i_ & 1) ? rwu : rwg, sw_ + (i_ >> 1) * 114688, ld_ + 32768 + (i_ & 1) * 8192 + (i_ >> 1) * 4096); \
          else glds(vox, rq, sx_ + (((i_ - 4) >> 1) + (i_ & 1) * 4) * 32768, ld_ + (((i_ - 4) >> 1) + (i_ & 1) * 4) * 4096); } } while (0)
      RANGE(0, 0, 12);
      const unsigned vod = VOD;
      WAITV(4); STAMP(16); BAR();
      RANGE(1, 0, 6);
      WAITV(6); BAR();
      RANGE(1, 6, 12); RANGE(2, 0, 6);
      tag = __builtin_amdgcn_readfirstlane(lds_r32(L_STAT + 2048));
      asm volatile("" : "+s"(sdp));
      const unsigned a0 = 32768 + (lw >> 1) * 8192 + (8 * g + qq) * 128 + (((2 * (lw & 1)) ^ ((qq >> 1) | ((g & 1) << 1))) << 5) + pp * 8, a1 = a0 ^ 32;
      const v4u f0 = lds_tr2(a0, a0 + 512), f1 = lds_tr2(a1, a1 + 512), f2 = lds_tr2(a0 + 4096, a0 + 4608), f3 = lds_tr2(a1 + 4096, a1 + 4608);
      WAITV(6); WAITL0(); STAMP(17); BAR();
      __builtin_amdgcn_s_setprio(2);
      RANGE(2, 6, 12);
      LD_SSM(lw); LD_SSM(w);
      PF(rwd, (l & 15) * 128, (kh * 896 + ((xcd >> 1) * 28 + r) * 8 + lw * 2) * 1024);
      RANGE(3, 0, 6);
      {
        const v4u one = {0x3f803f80u, 0x3f803f80u, 0x3f803f80u, 0x3f803f80u};
        const v4f c0 = mfma(f2, one, mfma(f0, one, v4f{0, 0, 0, 0})), c1 = mfma(f3, one, mfma(f1, one, v4f{0, 0, 0, 0}));
        const v4f cc = (l & 4) ? c1 : c0;
        const float csv = (l & 2) ? ((l & 1) ? cc[3] : cc[2]) : ((l & 1) ? cc[1] : cc[0]);
        st64(rws, (l & 8) ? 0x7ff00000u : WS_CS + (nt * 8 + mt) * 1024 + (lw * 32 + (l & 4) * 4 + 4 * g + (l & 3)) * 8, v2u{__float_as_uint(csv), tag});
      }
      WAITV(10); STAMP(18); BAR();
      RANGE(3, 6, 12); RANGE(4, 0, 10);
      WAITV(10); STAMP(19); BAR();
      v2u gm;
      v4u gc0, gc1;
      const unsigned om = WS_STAT + (mt * 256 + lt) * 16, oc = WS_CS + (nt * 8 + 2 * g) * 1024 + (lw * 16 + (l & 15)) * 16, oc1 = oc + 1024;
      RANGE(4, 10, 12); RANGE(5, 0, 12);
      WAITV(12); STAMP(20); BAR();
      ALD64(gm, om, rws); ALD128(gc0, oc, rws, 0); ALD128(gc1, oc1, rws, 0);
      RANGE(6, 0, 12);
      WAITV(15); STAMP(21); BAR();
      RANGE(7, 0, 4);
      WAITV(16); SB(); asm volatile("" : "+v"(gm), "+v"(gc0), "+v"(gc1));
      SPIN(gm.y == tag && gc0.y == tag && gc0.w == tag && gc1.y == tag && gc1.w == tag, RLD64(gm, om, rws); RLD128(gc0, oc, rws); RLD128(gc1, oc1, rws));
      lds_w32(L_STAT + lt * 4, gm.x ^ 0x80000000u);
      {
        const float c0 = xsum32(xsum16(add1(__uint_as_float(gc0.x), __uint_as_float(gc1.x)))), c1 = xsum32(xsum16(add1(__uint_as_float(gc0.z), __uint_as_float(gc1.z))));
        lds_w32(L_STAT + 1024 + (lw * 16 + (l & 15)) * 8 + (g & 1) * 4, __float_as_uint((g & 1) ? c1 : c0));
      }
      RANGE(7, 4, 12);
      WAITV(12); WAITL0(); STAMP(22); BAR();
      LD_D(lw, 0, 8);
      WAITV(8); STAMP(23); BAR();
      ALD32(p1, pa, rws);
      LD_D(lw, 8, 12);
      WAITV(4); SB(); asm volatile("" : "+v"(p1));
      SPIN(p1 == tag, RLD32(p1, pa, rws));
      LD_U();
      WAITV(0); ST_U();
      STAMP(2);
      BAR();
      __builtin_amdgcn_s_setprio(0);
    } else {
      __builtin_amdgcn_s_setprio(1);
      WAITV(0); ROWSTATS();
      asm volatile("" : "+s"(sdp));
      const unsigned mh = w >> 1, nh = w & 1;
      const unsigned ax0 = (128 * mh + (l & 15)) * 128 + ((g ^ ((l & 15) >> 1)) << 4), ax1 = ax0 ^ 64;
      const unsigned aw0 = 32768 + (8 * g + qq) * 128 + (((2 * nh) ^ ((qq >> 1) | ((g & 1) << 1))) << 5) + pp * 8, aw1 = aw0 ^ 32;
      v4f acc[8][4];
      sfor<32>([&](auto I) { acc[I.value >> 2][I.value & 3] = v4f{0, 0, 0, 0}; acc_pin<I.value>(acc[I.value >> 2][I.value & 3]); });
      v4u XA[4], XB[4], W[4][2];
#define RD_X(dst, i, P, sl) dst[i] = lds_r128(((i) & 1 ? ax1 : ax0) + (sl) + (2 * (P) + ((i) >> 1)) * 2048)
#define RD_W(n, ks, sl) W[n][ks] = lds_tr2(((n) & 1 ? aw1 : aw0) + (sl) + ((n) >> 1) * 8192 + (ks) * 4096, ((n) & 1 ? aw1 : aw0) + (sl) + ((n) >> 1) * 8192 + (ks) * 4096 + 512)
#define MM(X, P, n, i) acc_mfma<(2 * (P) + ((i) >> 1)) * 4 + (n)>(acc[2 * (P) + ((i) >> 1)][n], W[n][(i) & 1], X[i])
#define PAIR(X, P, RD, EX) sfor<16>([&](auto K_) { constexpr int k = K_.value; MM(X, P, k >> 2, k & 3); SB(); RD; EX; SB(); })
#define R0(sl) do { if constexpr (k < 2) RD_W(3, k, sl); else if constexpr (k < 6) RD_X(XB, k - 2, 1, sl); } while (0)
#define R1(sl) do { if constexpr (k < 4) RD_X(XA, k, 2, sl); } while (0)
#define R2(sl) do { if constexpr (k < 4) RD_X(XB, k, 3, sl); } while (0)
#define R3(sl) do { if constexpr (k < 4) RD_X(XA, k, 0, sl); else if constexpr ((k & 3) < 2) RD_W((k >> 2) - 1, k & 1, sl); } while (0)
      STAMP(16);
      WAITL0(); BAR();
      STAMP(1);
      RD_W(0, 0, 0); RD_X(XA, 0, 0, 0); RD_W(0, 1, 0); RD_X(XA, 1, 0, 0); RD_X(XA, 2, 0, 0); RD_X(XA, 3, 0, 0);
      RD_W(1, 0, 0); RD_W(1, 1, 0); RD_W(2, 0, 0); RD_W(2, 1, 0);
      SB();
      unsigned cur = 0;
      for (unsigned s = 0; s < 6; ++s) {
        PAIR(XA, 0, R0(cur), NOP_);
        if (s == 0) BAR();
        PAIR(XB, 1, R1(cur), NOP_); PAIR(XA, 2, R2(cur), NOP_);
        cur = cur == 2 * SLOT ? 0 : cur + SLOT;
        WAITL0(); BAR();
        PAIR(XB, 3, R3(cur), NOP_);
      }
      const unsigned acs = pin(L_STAT + 1024 + nh * 128 + g * 16), am = pin(L_STAT + mh * 512 + (l & 15) * 4);
      const unsigned ks1 = nt * 2 + nh;
      const unsigned ah0 = pin(WS_H + ((mt * 4 + mh * 2) * 56 + ks1) * 4096 + l * 16), ah1 = pin(ah0 + 56 * 4096);
      v4f cs[4];
      float mm[8];
#define RS do { if constexpr (k >= 6 && k < 10) { mm[(2 * k - 6) & 7] = __uint_as_float(lds_r32(am + ((2 * k - 6) & 7) * 64)); mm[(2 * k - 5) & 7] = __uint_as_float(lds_r32(am + ((2 * k - 5) & 7) * 64)); \
          cs[k - 6] = __builtin_bit_cast(v4f, lds_r128(acs + ((k - 6) >> 1) * 256 + ((k - 6) & 1) * 64)); } } while (0)
#define FIX1(P, j) acc_fma<(2 * (P) + ((j) & 1)) * 4 + (((j) >> 1) & 3), ((j) >> 3)>(acc[2 * (P) + ((j) & 1)][((j) >> 1) & 3], mm[2 * (P) + ((j) & 1)], cs[((j) >> 1) & 3][(j) >> 3])
#define FIXN(P, first, cnt) sfor<(cnt)>([&](auto J_) { FIX1(P, (first) + J_.value); })
#define FIXA(P) do { if constexpr (k < 4) FIXN(P, k, 1); else if constexpr (k < 12) FIXN(P, 2 * k - 4, 2); else FIXN(P, 3 * k - 16, 3); } while (0)
#define FIXB(P) do { if constexpr (k < 4) FIXN(P, 2 * k, 2); else if constexpr ((k & 3) < 2) FIXN(P, 2 * k - (k & 1), 1); else FIXN(P, 2 * k - 2 + (k & 1), 3); } while (0)
#define FIXC(P) do { if constexpr (k >= 2 && k < 6) FIXN(P, k - 2, 1); else if constexpr (k >= 6 && k < 8) FIXN(P, 2 * k - 8, 2); else if constexpr (k >= 8) FIXN(P, 3 * k - 16, 3); } while (0)
#define PAIR2(X, P, RD, EX) sfor<16>([&](auto K_) { constexpr int k = K_.value; MM(X, P, k >> 2, ((k & 1) << 1) | ((k >> 1) & 1)); SB(); RD; EX; SB(); })
      PAIR(XA, 0, R0(0), RS); PAIR2(XB, 1, R1(0), FIXA(3)); PAIR2(XA, 2, R2(0), FIXA(0));
      WAITL0(); BAR();
      PAIR2(XB, 3, R3(SLOT), FIXB(1));
      STAMP(2);
      unsigned ho[4];
      float t0, t1;
#define OUTA(i, e) glu2a(t0, t1, acc[i][(e) >> 1][((e) & 1) * 2], acc[i][(e) >> 1][((e) & 1) * 2 + 1])
#define OUTB(i, e) do { ho[e] = glu2b(t0, t1, acc[i][2 + ((e) >> 1)][((e) & 1) * 2], acc[i][2 + ((e) >> 1)][((e) & 1) * 2 + 1]); \
        if ((e) == 3) st128(rws, ((i) < 4 ? ah0 : ah1) + ((i) & 3) * 1024, 0, v4u{ho[0], ho[1], ho[2], ho[3]}); } while (0)
#define OUT(P) do { if constexpr (k & 1) OUTB(2 * (P) + (k >> 3), (k >> 1) & 3); else OUTA(2 * (P) + (k >> 3), (k >> 1) & 3); } while (0)
      PAIR2(XA, 0, R0(SLOT), FIXC(2)); PAIR2(XB, 1, R1(SLOT), OUT(0)); PAIR2(XA, 2, R2(SLOT), OUT(1));
      WAITL0(); BAR();
      PAIR2(XB, 3, NOP_, OUT(2));
      STAMP(3);
      asm volatile("s_nop 7" ::: "memory");
      SB();
#pragma unroll
      for (int k = 0; k < 8; ++k) { OUTA(6 + (k >> 2), k & 3); OUTB(6 + (k >> 2), k & 3); SB(); }
      hflag = WS_FH + ((mt * 2 + mh) * 56 + ks1) * 128;

    }
  } else {
    const unsigned al = r - 28, a = xcd * 4 + al;
    const rsrc_t rk = mk(p.k, 2u << 20), rv = mk(p.v, 2u << 20), rsk = mk(p.sk, 4096);
    const unsigned vod = VOD;
    if (w >= 4) glds(l * 16, rsk, (w - 4) * 1024, 69632 + (w - 4) * 1024);
    v4u kk[11], vv[8];
    const int t0 = a * 64 + w * 8;
#pragma unroll
    for (int i = 0; i < 11; ++i) {
      const int t = t0 - 3 + i;
      kk[i] = ld128p(rk, l * 16 + (t < 0 ? 0x7ff00000u : (unsigned)t * 1024), 0);
      if (i >= 3) vv[i - 3] = ld128p(rv, l * 16, (t0 + i - 3) * 1024);
      SB();
      if (i == 3) { WAITV(5); STAMP(16); BAR(); }
      if (i == 6) break;
    }
    if (w < 4) ROWSTATS();
    asm volatile("" : "+s"(sdp));
    v2f tp[4][4], kf[4][4], prev[4];
    v4u o[8];
#pragma unroll
    for (int j = 0; j < 4; ++j) {
      const v4u sk = lds_r128(69632 + l * 64 + j * 16);
      tp[j][0] = v2f{lo(sk[0]), lo(sk[2])}; tp[j][1] = v2f{hi(sk[0]), hi(sk[2])}; tp[j][2] = v2f{lo(sk[1]), lo(sk[3])}; tp[j][3] = v2f{hi(sk[1]), hi(sk[3])};
    }
#pragma unroll
    for (int i = 0; i < 11; ++i) {
#pragma unroll
      for (int j = 0; j < 4; ++j) kf[i & 3][j] = v2f{lo(kk[i][j]), hi(kk[i][j])};
      if (i >= 3) {
#pragma unroll
        for (int j = 0; j < 4; ++j) {
          const v2f y = __builtin_elementwise_fma(kf[i & 3][j], tp[j][0], __builtin_elementwise_fma(kf[(i - 1) & 3][j], tp[j][1],
                          __builtin_elementwise_fma(kf[(i - 2) & 3][j], tp[j][2], kf[(i - 3) & 3][j] * tp[j][3]))) * v2f{lo(vv[i - 3][j]), hi(vv[i - 3][j])};
          if (i & 1) prev[j] = y;
          else { o[2 * j][(i - 4) >> 1] = pk(prev[j].x, y.x); o[2 * j + 1][(i - 4) >> 1] = pk(prev[j].y, y.y); }
        }
      }
      SB();
      if (i == 2) STAMP(17);
      if (i == 3) STAMP(18);
      if (i == 6) STAMP(19);
      if (i == 8) STAMP(20);
      if (i == 10) STAMP(21);
      if (i >= 3 && i < 7) { kk[i + 4] = ld128p(rk, l * 16, (t0 + i + 1) * 1024); vv[i + 1] = ld128p(rv, l * 16, (t0 + i + 1) * 1024); SB(); }
    }
#pragma unroll
    for (int j = 0; j < 8; ++j) lds_w128((8 * l + j) * 128 + ((w ^ (l & 7)) << 4), o[j]);
    STAMP(1);
    WAITL0(); BAR();
    tag = __builtin_amdgcn_readfirstlane(lds_r32(L_STAT + 2048));
#pragma unroll
    for (int i = 0; i < 8; ++i) st128(rws, l * 16, WS_U + a * 65536 + (8 * w + i) * 1024, lds_r128((64 * w + 8 * i + (l >> 3)) * 128 + (((l & 7) ^ i) << 4)));
    SB();
    STAMP(22);
    if (w < 4) {
      LD_D(lw, 0, 2);
      WAITV(2); BAR();
      if (tid == 0) st32(rws, WS_FA + a * 128, tag);
      LD_D(lw, 2, 6);
      BAR();
      LD_D(lw, 6, 12);
    } else {
      LD_SSM(lw); LD_SSM(w);
      WAITV(2); BAR();
      WAITV(0); BAR();
      STAMP(2);
      RLD32(p1, pa, rws);
      SPIN(p1 == tag, RLD32(p1, pa, rws));
      LD_U();
      WAITV(0); ST_U();
    }
  }
  STAMP(4);
#define RD_D() _Pragma("unroll") for (int n = 0; n < 4; ++n) { \
    const unsigned a0_ = pin(PIECE(lw, 0) + ADR(n)), a1_ = pin(PIECE(lw, 8) + ADR(n)), a2_ = pin(PIECE(lw, 20) + ADR(n)); \
    _Pragma("unroll") for (int ks = 0; ks < 7; ++ks) { const unsigned aa = (ks < 2 ? a0_ : ks < 5 ? a1_ : a2_) + (ks < 2 ? ks : ks < 5 ? ks - 2 : ks - 5) * 4096; WD[ks][n] = lds_tr2(aa, aa + 2048); } }
  if (w < 4) {
    WAITV0();
    st32(rws, l == 0 ? hflag : 0x7ff00000u, tag);
    __builtin_amdgcn_s_setprio(0);
    STAMP(5);
    RLD32(p2, oh, rws);
    STAMP(6);
    SPIN(p2 == tag, RLD32(p2, oh, rws));
    STAMP(7);
    LD_H(0);
    BAR();
    RD_D();
    SB();
    LD_H(1);
  } else {
    const unsigned ch = lw >> 1, par = lw & 1, vod = VOD;
    const unsigned aH = pin(L_SP + ch * 4128 + 224 + 8 * pp - 64 * g - 8 * qq);
    const unsigned aU = pin(L_U + ch * 4320 + 128 * par + 32 * pp + 64 * g + 8 * qq);
    const unsigned aY = pin(L_Y + ch * 8192 + par * 256 + l * 4);
    const unsigned aC = pin(g == 3 ? L_CARRY + ch * 528 + (par + 1) * 16 + qq * 4 : L_DUMMY);
    v4u H[16], U[16];
    v4f ya[16];
    int kc[16];
#define RD_H(i) H[i] = lds_tr2(aH + 256 * (i) + 32, aH + 256 * (i))
#define RD_U(i) U[i] = lds_tr2(aU + 256 * (i), aU + 256 * (i) + 32)
#define EPI(v, e) do { \
      if ((e) == 0) ya[v][2] += shr1(ya[v][3]); \
      if ((e) == 1) ya[v][1] += shr1(ya[v][2]); \
      if ((e) == 2) { ya[v][0] += shr1(ya[v][1]); kc[v] = __builtin_amdgcn_update_dpp(0, __float_as_int(ya[v][1]), 0x15f, 0xf, 0xf, true); } \
      if ((e) == 3) { lds_w32(aY + (v) * 512, __float_as_uint(ya[v][0])); kc[v] = __builtin_amdgcn_update_dpp(kc[v], __float_as_int(ya[v][2]), 0x15f, 0xf, 0xe, false); } \
      if ((e) == 4) kc[v] = __builtin_amdgcn_update_dpp(kc[v], __float_as_int(ya[v][3]), 0x15f, 0xf, 0xc, false); \
      if ((e) == 5) lds_w32(aC + (v) * 32, kc[v]); } while (0)
    WAITL0();
#pragma unroll
    for (int i = 0; i < 4; ++i) { RD_U(i); RD_H(i); }
    SB();
    int ecnt = 0, dcnt = 12;
#pragma unroll
    for (int v = 0; v < 16; ++v) {
#pragma unroll
      for (int z = 0; z <= v; ++z) {
        if (z) mfma_acc(ya[v], U[v - z], H[z]); else mfma_new(ya[v], U[v], H[0]);
        SB();
        if (z == 0 && v + 4 < 16) { RD_U(v + 4); if (v == 0) RD_H(4); }
        else if (z == 1 && v + 4 < 16) RD_H(v + 4);
        else if (v >= D_AT && dcnt < 28 && (z & 1)) { LD_D(lw, dcnt, dcnt + 1); ++dcnt; }
        else if (v >= 4 && (ecnt / 6 < v - 1 || (ecnt / 6 == v - 1 && z >= 2))) { EPI(ecnt / 6, ecnt % 6); ++ecnt; }
        SB();
      }
      if (v == X0_AT) { WAITV(0); BAR(); }
      if (v == 14) ALD32(p2, oh, rws);
    }
    asm volatile("s_nop 7\n\ts_nop 3" : "+v"(ya[15]));
    STAMP(5);
#pragma unroll
    for (int e = 0; e < 96; ++e) if (e >= ecnt) EPI(e / 6, e % 6);
    SB();
    WAITL0(); BAR();
    WAITV(0); SB(); asm volatile("" : "+v"(p2));
    STAMP(6);
    SPIN(p2 == tag, RLD32(p2, oh, rws));
    STAMP(7);
    LD_H(0);
    SB();
    RD_D();
    SB();
    LD_H(1);
  }
  SB();
  STAMP(8);
  const unsigned aW0 = w * 4096 + ((l ^ ((g >> 1) << 2)) << 4), aW1 = aW0 ^ 128;
#define REDB(st) ((st) == 0 ? 2 * SLOT : ((st) - 1) * 32768)
  v4f oa[4], ob[4];
#define WR_P(o, st, n) lds_w128(REDB(st) + (((n) & 1) ? aW1 : aW0) + (n) * 1024, __builtin_bit_cast(v4u, o[n]))
#define G2(o, st, FILL) do { \
    _Pragma("unroll") for (int ks = 0; ks < 7; ++ks) { \
      _Pragma("unroll") for (int n = 0; n < 4; ++n) { o[n] = mfma(WD[ks][n], Hf[st][ks], ks ? o[n] : v4f{0, 0, 0, 0}); SB(); FILL; SB(); } } } while (0)
  G2(oa, 0, if (n == 3) LD_H1(2, ks));
  if (w < 4) {
    BAR();
    const float d0 = lo(sdp), d1 = hi(sdp);
    const unsigned by = pin(L_Y + tid * 8), bu = pin(L_U + 224 + tid * 4), bz = pin(WS_Z + b * 16384 + tid * 16);
    const unsigned cstep = (l & 31) < 2 ? 128 : 0, bc = L_CARRY + ((l & 31) < 2 ? (tid >> 5) * 16 + (l & 31) * 8 : 0);
#pragma unroll
    for (int hh = 0; hh < 2; ++hh) {
      v2f y0[2], y1[2], c0[2], c1[2];
      unsigned u0[2], u1[2];
#pragma unroll
      for (int i = 0; i < 2; ++i) {
        const unsigned q = 2 * hh + i, ca = pin(bc + q * cstep);
        y0[i] = __builtin_bit_cast(v2f, lds_r64(by + q * 2048)); c0[i] = __builtin_bit_cast(v2f, lds_r64(ca));
        y1[i] = __builtin_bit_cast(v2f, lds_r64(by + 8192 + q * 2048)); c1[i] = __builtin_bit_cast(v2f, lds_r64(ca + 528));
        u0[i] = lds_r32(bu + q * 1024); u1[i] = lds_r32(bu + 4320 + q * 1024);
      }
      SB();
#pragma unroll
      for (int i = 0; i < 2; ++i)
        st128(rws, bz + (2 * hh + i) * 4096, 0, v4u{pk(fmaf(lo(u0[i]), d0, y0[i].x + c0[i].x), fmaf(lo(u1[i]), d1, y1[i].x + c1[i].x)), tag,
            pk(fmaf(hi(u0[i]), d0, y0[i].y + ((l & 1) ? 0.f : c0[i].y)), fmaf(hi(u1[i]), d1, y1[i].y + ((l & 1) ? 0.f : c1[i].y))), tag});
      SB();
    }
  }
  SB();
  STAMP(9);
  WAITL0(); BAR();
  G2(ob, 1, if (n == 3) LD_H1(3, ks); if (n == 1 && ks < 4) WR_P(oa, 0, ks));
#pragma unroll
  for (int n = 0; n < 4; ++n) WR_P(ob, 1, n);
  STAMP(10);
  const unsigned tok = (w & 1) * 8 + (l >> 3), cg = l & 7, stp = ((w >> 1) & 1) ^ rot;
  const unsigned aR = (w >> 2) * 16384 + (cg >> 1) * 1024 + ((((cg & 1) * 32 + tok) ^ ((cg & 3) << 2)) << 4);
  const unsigned aR0 = pin(aR + (stp ? REDB(1) : REDB(0))), aR1 = pin(aR + (stp ? REDB(3) : REDB(2)));
  const unsigned row = rt * 64 + w * 8 + (l >> 3);
  const unsigned oz = WS_Z + (ct * 32 + w * 4 + (l >> 5)) * 16384 + rt * 512 + (l & 31) * 16, oz1 = oz + 32768, ogs = WS_STAT + row * 16;
  const unsigned op = WS_P + (b ^ 1) * 16384 + tid * 16, op1 = op + 8192;
  v4f pp_[8];
  float sm[8];
#define RD_P(base) _Pragma("unroll") for (int i = 0; i < 8; ++i) pp_[i] = __builtin_bit_cast(v4f, lds_r128((base) + (i & 3) * 4096 + (i >> 2) * 256))
#define SUM4(j) sm[j] = add1(add1(pp_[(j) & 4][(j) & 3], pp_[((j) & 4) + 1][(j) & 3]), add1(pp_[((j) & 4) + 2][(j) & 3], pp_[((j) & 4) + 3][(j) & 3]))
  WAITL0(); BAR();
  RD_P(aR0);
  SB();
#pragma unroll
  for (int j = 0; j < 8; ++j) SUM4(j);
  st128(rws, tid * 16, WS_P + b * 16384, v4u{pk(sm[0], sm[1]), tag, pk(sm[2], sm[3]), tag});
  st128(rws, tid * 16, WS_P + b * 16384 + 8192, v4u{pk(sm[4], sm[5]), tag, pk(sm[6], sm[7]), tag});
  SB();
#define FILL2 do { const int c_ = ks * 4 + n; \
    if (c_ == 7) xv = ld128p(rq, row * 1024 + cg * 16, ct * 128); \
    if (c_ == 11) gs = ld128(rws, ogs, 0); \
    if (c_ == 15) z0 = ld128(rws, oz, 0); \
    if (c_ == 19) z1 = ld128(rws, oz1, 0); } while (0)
  v4u xv, gs, z0, z1, q0, q1;
  G2(oa, 2, FILL2);
  STAMP(11);
  G2(ob, 3, if (n == 1 && ks < 4) WR_P(oa, 2, ks); if (n == 3 && ks == 5) q0 = ld128(rws, op, 0); if (n == 3 && ks == 6) q1 = ld128(rws, op1, 0));
#pragma unroll
  for (int n = 0; n < 4; ++n) WR_P(ob, 3, n);
  STAMP(12);
  SPIN(z0.y == tag && z0.w == tag && z1.y == tag && z1.w == tag && gs.y == tag && gs.w == tag, RLD128X2(z0, oz, z1, oz1, rws); RLD128(gs, ogs, rws));
  {
    const unsigned za = pin(L_SP + ((l & 31) * 2 * 33 + w * 4 + (l >> 5)) * 4);
    lds_w32(za, z0.x); lds_w32(za + 132, z0.z); lds_w32(za + 8, z1.x); lds_w32(za + 140, z1.z);
  }
  const unsigned zr = pin(L_SP + ((row & 63) * 33 + cg * 4) * 4);
  const float rstd = __uint_as_float(gs.z), nm = -__uint_as_float(gs.x) * rstd, r2 = rstd * rstd;
  float xf[8], xn[8];
#pragma unroll
  for (int j = 0; j < 8; ++j) { xf[j] = (j & 1) ? hi(xv[j >> 1]) : lo(xv[j >> 1]); xn[j] = fma1(xf[j], rstd, nm); asm volatile("" : "+v"(xf[j]), "+v"(xn[j])); }
  SPIN(q0.y == tag && q0.w == tag && q1.y == tag && q1.w == tag, RLD128X2(q0, op, q1, op1, rws));
#pragma unroll
  for (int j = 0; j < 8; ++j) { const unsigned qd = j < 2 ? q0.x : j < 4 ? q0.z : j < 6 ? q1.x : q1.z; xf[j] = fma1(r2, (j & 1) ? hi(qd) : lo(qd), xf[j]); asm volatile("" : "+v"(xf[j])); }
  WAITL0(); BAR();
  STAMP(13);
  {
    unsigned zz[4];
#pragma unroll
    for (int j = 0; j < 4; ++j) zz[j] = lds_r32(zr + j * 4);
    RD_P(aR1);
    SB();
    const v4f s0 = (pp_[0] + pp_[1]) + (pp_[2] + pp_[3]), s1 = (pp_[4] + pp_[5]) + (pp_[6] + pp_[7]);
    float e[8];
#pragma unroll
    for (int j = 0; j < 8; ++j) e[j] = fmaf(r2, j < 4 ? s0[j & 3] : s1[j & 3], fmaf(xn[j], (j & 1) ? hi(zz[j >> 1]) : lo(zz[j >> 1]), xf[j]));
    st128(mk(p.out, 2u << 20), row * 1024 + cg * 16, ct * 128, v4u{pk(e[0], e[1]), pk(e[2], e[3]), pk(e[4], e[5]), pk(e[6], e[7])});
  }
  if (b == 0 && tid == 448) st32(rws, WS_EPOCH, tag);
  STAMP(14);
}

extern "C" void kernel_launch(void* const* d_in, const int* in_sizes, int n_in, void* d_out, int out_size, void* d_ws, size_t ws_size,
                              hipStream_t stream) {
  Params p;
  p.q = (const u16*)d_in[0]; p.k = (const u16*)d_in[1]; p.v = (const u16*)d_in[2]; p.sk = (const u16*)d_in[3];
  p.ssm = (const u16*)d_in[4]; p.sd = (const u16*)d_in[5]; p.wg = (const u16*)d_in[7]; p.wu = (const u16*)d_in[8];
  p.wd = (const u16*)d_in[9]; p.out = (u16*)d_out; p.ws = (char*)d_ws;
  (void)hipFuncSetAttribute((const void*)fused_kernel, hipFuncAttributeMaxDynamicSharedMemorySize, LDS_BYTES);
  fused_kernel<<<256, 512, LDS_BYTES, stream>>>(p);
}
```
